```python
import torch

def _softmax(x, axis=-1):
    return torch.softmax(x, dim=axis)
def _gelu(x):
    return torch.nn.functional.gelu(x, approximate="tanh")
def _silu(x):
    return torch.nn.functional.silu(x)
def _sigmoid(x):
    return torch.sigmoid(x)
def _layernorm(x, axis=-1, eps=1e-5):
    m = x.mean(dim=axis, keepdim=True)
    v = x.var(dim=axis, keepdim=True, unbiased=False)
    return (x - m) / torch.sqrt(v + eps)
def _scatter_add(tgt, idx, src, axis=0):
    return tgt.clone().index_add_(axis, idx, src)
def _segment_sum(src, seg, n):
    out = torch.zeros((n,) + src.shape[1:], dtype=src.dtype)
    return out.index_add_(0, seg, src)
def _pad(x, pw):
    flat = tuple(v for pair in reversed(pw) for v in pair)
    return torch.nn.functional.pad(x, flat)

kernel_name = 'gated_mix__linear_kernel_hedgehog__sliding_block__prompt'

def setup_inputs():

    _w = (lambda a: (lambda t: t.to(torch.bfloat16) if t.is_floating_point() else t)(torch.as_tensor(a)))
    def _A_setup():
        B, S, D, H = 1, 2048, 576, 32
        Dh = D // H
        return {
            "x": _w((lambda *s: torch.randn(*s, dtype=torch.bfloat16) * 0.05)(B, S, D)),
            "wqkv": _w((lambda *s: torch.randn(*s, dtype=torch.bfloat16) * 1.6)(D, 3 * D)),
            "w_phi": _w((lambda *s: torch.randn(*s, dtype=torch.bfloat16) * 1.6)(H, Dh, Dh) * (Dh ** -0.5)),
            "wo": _w((lambda *s: torch.randn(*s, dtype=torch.bfloat16) * 0.1)(D, D)),
        }
    _A_in = _A_setup()

    def _B_setup():
        B, S, D = 1, 2048, 576
        return {
            "x": _w((lambda *s: torch.randn(*s, dtype=torch.bfloat16) * 0.05)(B, S, D)),
            "wqkv": _w((lambda *s: torch.randn(*s, dtype=torch.bfloat16) * 0.8)(D, 3 * D)),
            "wo": _w((lambda *s: torch.randn(*s, dtype=torch.bfloat16) * 0.05)(D, D)),
        }
    _B_in = _B_setup()

    return {
        "A_x": _A_in["x"],
        "A_wqkv": _A_in["wqkv"],
        "A_w_phi": _A_in["w_phi"],
        "A_wo": _A_in["wo"],
        "B_x": _B_in["x"],
        "B_wqkv": _B_in["wqkv"],
        "B_wo": _B_in["wo"],
        "gate_w": _w((lambda *s: torch.randn(*s, dtype=torch.bfloat16) * 0.05)(576) * 16.0),
    }

def reference(A_x, A_wqkv, A_w_phi, A_wo, B_x, B_wqkv, B_wo, gate_w):
    def _A_ref(x, wqkv, w_phi, wo):
        B, S, D = x.shape
        H = 32; Dh = D // H
        qkv = (lambda x, *d: x.permute(*d))((x @ wqkv).reshape(B, S, 3, H, Dh), 2, 0, 3, 1, 4)
        q, k, v = qkv[0], qkv[1], qkv[2]
        pq = torch.einsum("bhsd,hdf->bhsf", q, w_phi)
        pk = torch.einsum("bhsd,hdf->bhsf", k, w_phi)
        phi_q = torch.concatenate([_softmax(pq, axis=-1), _softmax(-pq, axis=-1)], axis=-1)
        phi_k = torch.concatenate([_softmax(pk, axis=-1), _softmax(-pk, axis=-1)], axis=-1)
        kv = torch.einsum("bhsf,bhsd->bhfd", phi_k, v)
        z  = torch.einsum("bhsf->bhf", phi_k)
        num = torch.einsum("bhqf,bhfd->bhqd", phi_q, kv)
        den = torch.einsum("bhqf,bhf->bhq", phi_q, z)[..., None] + 1e-6
        o = num / den
        return (lambda x, *d: x.permute(*d))(o, 0, 2, 1, 3).reshape(B, S, D) @ wo

    def _B_ref(x, wqkv, wo):
        B, S, D = x.shape
        H = 32; Dh = D // H; W = 256
        qkv = (lambda x, *d: x.permute(*d))((x @ wqkv).reshape(B, S, 3, H, Dh), 2, 0, 3, 1, 4)
        q, k, v = qkv[0], qkv[1], qkv[2]
        scores = torch.einsum("bhqd,bhkd->bhqk", q, k) * (Dh ** -0.5)
        idx = torch.arange(S)
        mask = (idx[:, None] - idx[None, :] >= 0) & (idx[:, None] - idx[None, :] < W)
        scores = torch.where(mask[None, None], scores, -1e9)
        p = _softmax(scores, axis=-1)
        o = torch.einsum("bhqk,bhkd->bhqd", p, v)
        return (lambda x, *d: x.permute(*d))(o, 0, 2, 1, 3).reshape(B, S, D) @ wo

    x = A_x
    g = _sigmoid((x * gate_w).sum(-1, keepdims=True))
    a = _A_ref(x, A_wqkv, A_w_phi, A_wo)
    b = _B_ref(x, B_wqkv, B_wo)
    return g * a + (1 - g) * b
```

```cpp
#include <hip/hip_runtime.h>
#include <hip/hip_bf16.h>

typedef __hip_bfloat16 bf16;
constexpr int S = 2048, D = 576, H = 32, DH = 18, W = 256;

__device__ __forceinline__ float b2f(bf16 x) { return __bfloat162float(x); }

__global__ void k_qkv(const bf16* x, const bf16* wA, const bf16* wB, float* qkv) {
    int n = blockIdx.x * blockDim.x + threadIdx.x;
    int s0 = blockIdx.y * 8;
    if (n >= 3456) return;
    const bf16* w = n < 1728 ? wA + n : wB + (n - 1728);
    float acc[8] = {0};
    for (int k = 0; k < D; ++k) {
        float wv = b2f(w[(size_t)k * 1728]);
#pragma unroll
        for (int i = 0; i < 8; ++i) acc[i] += b2f(x[(size_t)(s0 + i) * D + k]) * wv;
    }
#pragma unroll
    for (int i = 0; i < 8; ++i) qkv[(size_t)(s0 + i) * 3456 + n] = __bfloat162float(__float2bfloat16(acc[i]));
}

__global__ __launch_bounds__(256) void k_phi(const float* qkv, const bf16* wphi, float* phiq, float* phik) {
    int idx = blockIdx.x * blockDim.x + threadIdx.x;
    if (idx >= S * H * 2) return;
    int which = idx & 1; int h = (idx >> 1) % H; int s = idx / (2 * H);
    const float* src = qkv + (size_t)s * 3456 + which * 576 + h * DH;
    float p[DH];
    float mx = -1e30f, mn = 1e30f;
#pragma unroll
    for (int f = 0; f < DH; ++f) {
        float a = 0;
#pragma unroll
        for (int d = 0; d < DH; ++d) a += src[d] * b2f(wphi[(h * DH + d) * DH + f]);
        p[f] = a; mx = fmaxf(mx, a); mn = fminf(mn, a);
    }
    float sp = 0, sn = 0;
#pragma unroll
    for (int f = 0; f < DH; ++f) { sp += __expf(p[f] - mx); sn += __expf(mn - p[f]); }
    float* dst = (which ? phik : phiq) + ((size_t)s * H + h) * 36;
#pragma unroll
    for (int f = 0; f < DH; ++f) { dst[f] = __expf(p[f] - mx) / sp; dst[18 + f] = __expf(mn - p[f]) / sn; }
}

__global__ void k_kv(const float* qkv, const float* phik, float* kv) {
    int h = blockIdx.x; int t = threadIdx.x;
    if (t >= 36 * 19) return;
    int f = t / 19, d = t % 19;
    float a = 0;
    for (int s = 0; s < S; ++s) {
        float v = d < 18 ? qkv[(size_t)s * 3456 + 1152 + h * DH + d] : 1.f;
        a += phik[((size_t)s * H + h) * 36 + f] * v;
    }
    kv[(h * 36 + f) * 19 + d] = a;
}

__global__ __launch_bounds__(256) void k_attn(const float* qkv, const float* phiq, const float* kv, const bf16* x, const bf16* gw, float* o) {
    int idx = blockIdx.x * blockDim.x + threadIdx.x;
    if (idx >= S * H) return;
    int h = idx % H, s = idx / H;
    float g = 0;
    for (int k = 0; k < D; ++k) g += b2f(x[(size_t)s * D + k]) * b2f(gw[k]);
    g = 1.f / (1.f + __expf(-g));
    float num[19];
#pragma unroll
    for (int d = 0; d < 19; ++d) num[d] = 0;
    for (int f = 0; f < 36; ++f) {
        float p = phiq[((size_t)s * H + h) * 36 + f];
#pragma unroll
        for (int d = 0; d < 19; ++d) num[d] += p * kv[(h * 36 + f) * 19 + d];
    }
    float inv = 1.f / (num[18] + 1e-6f);
#pragma unroll
    for (int d = 0; d < 18; ++d) o[(size_t)s * 1152 + h * DH + d] = g * num[d] * inv;
    const float* q = qkv + (size_t)s * 3456 + 1728 + h * DH;
    float qq[18];
#pragma unroll
    for (int d = 0; d < 18; ++d) qq[d] = q[d] * 0.23570226f;
    int k0 = s - (W - 1); if (k0 < 0) k0 = 0;
    float mx = -1e30f;
    for (int j = k0; j <= s; ++j) {
        const float* kk = qkv + (size_t)j * 3456 + 1728 + 576 + h * DH;
        float a = 0;
#pragma unroll
        for (int d = 0; d < 18; ++d) a += qq[d] * kk[d];
        mx = fmaxf(mx, a);
    }
    float l = 0; float acc[18];
#pragma unroll
    for (int d = 0; d < 18; ++d) acc[d] = 0;
    for (int j = k0; j <= s; ++j) {
        const float* kk = qkv + (size_t)j * 3456 + 1728 + 576 + h * DH;
        const float* vv = kk + 576;
        float a = 0;
#pragma unroll
        for (int d = 0; d < 18; ++d) a += qq[d] * kk[d];
        float p = __expf(a - mx); l += p;
#pragma unroll
        for (int d = 0; d < 18; ++d) acc[d] += p * vv[d];
    }
    float il = (1.f - g) / l;
#pragma unroll
    for (int d = 0; d < 18; ++d) o[(size_t)s * 1152 + 576 + h * DH + d] = acc[d] * il;
}

__global__ void k_out(const float* o, const bf16* woA, const bf16* woB, bf16* out) {
    int n = blockIdx.x * blockDim.x + threadIdx.x;
    int s0 = blockIdx.y * 8;
    if (n >= D) return;
    float acc[8] = {0};
    for (int k = 0; k < 1152; ++k) {
        float wv = b2f(k < 576 ? woA[(size_t)k * D + n] : woB[(size_t)(k - 576) * D + n]);
#pragma unroll
        for (int i = 0; i < 8; ++i) acc[i] += o[(size_t)(s0 + i) * 1152 + k] * wv;
    }
#pragma unroll
    for (int i = 0; i < 8; ++i) out[(size_t)(s0 + i) * D + n] = __float2bfloat16(acc[i]);
}

extern "C" void kernel_launch(void* const* d_in, const int* in_sizes, int n_in,
                              void* d_out, int out_size, void* d_ws, size_t ws_size,
                              hipStream_t stream) {
    const bf16* x = (const bf16*)d_in[0];
    const bf16* wqkvA = (const bf16*)d_in[1];
    const bf16* wphi = (const bf16*)d_in[2];
    const bf16* woA = (const bf16*)d_in[3];
    const bf16* wqkvB = (const bf16*)d_in[5];
    const bf16* woB = (const bf16*)d_in[6];
    const bf16* gw = (const bf16*)d_in[7];
    float* ws = (float*)d_ws;
    float* qkv = ws;
    float* phiq = qkv + (size_t)S * 3456;
    float* phik = phiq + (size_t)S * H * 36;
    float* kv = phik + (size_t)S * H * 36;
    float* o = kv + H * 36 * 19 + 64;
    k_qkv<<<dim3(3456 / 128, S / 8), 128, 0, stream>>>(x, wqkvA, wqkvB, qkv);
    k_phi<<<S * H * 2 / 256, 256, 0, stream>>>(qkv, wphi, phiq, phik);
    k_kv<<<H, 704, 0, stream>>>(qkv, phik, kv);
    k_attn<<<S * H / 256, 256, 0, stream>>>(qkv, phiq, kv, x, gw, o);
    k_out<<<dim3(5, S / 8), 128, 0, stream>>>(o, woA, woB, (bf16*)d_out);
}
```

```cpp
#include <hip/hip_runtime.h>
#include <stdint.h>

#define LAS __attribute__((address_space(3)))
#define GAS __attribute__((address_space(1)))
typedef short bf16x8 __attribute__((ext_vector_type(8)));
typedef short bf16x4 __attribute__((ext_vector_type(4)));
typedef float f32x2 __attribute__((ext_vector_type(2)));
typedef float f32x4 __attribute__((ext_vector_type(4)));
typedef float f32x16 __attribute__((ext_vector_type(16)));
typedef unsigned u32x2 __attribute__((ext_vector_type(2)));
typedef unsigned u32x4 __attribute__((ext_vector_type(4)));
typedef __bf16 bf2_t __attribute__((ext_vector_type(2)));
typedef unsigned long long u64;

constexpr int S = 2048, D = 576;
constexpr float LOG2E = 1.4426950408889634f;
constexpr float QSCALE_B = 0.23570226039551584f * LOG2E;
constexpr float NEG = -1.0e30f;

constexpr size_t WS_EPOCH = 0, WS_FKV = 4096, WS_FKP = 20480, WS_FO = 36864, WS_TMO = 69632;
constexpr size_t WS_KG = 131072;
constexpr int KG_BYTES = 43008, KG_K = 24576;
constexpr size_t WS_KVP = 6291456;
constexpr int KVP_BYTES = 12288, KVP_CHUNKS = 760;
constexpr size_t WS_O = 8388608;
constexpr size_t WS_STAMP = 16777216;

constexpr int XS = 16384, WSB = 27648, BUFS = XS + WSB;
constexpr int L_K = 0;
constexpr int L_V = 73728, VROW = 784;
constexpr int L_Q = 130960;
constexpr int L_G = 155648;
constexpr int L_GW = 158720;
constexpr int L_WPHI = 160768;
constexpr int LDS_BYTES = 163840;
constexpr unsigned SPIN_CAP = 1u << 18;

struct Params {
    const void* x; const void* wqkvA; const void* wphi; const void* woA; const void* wqkvB; const void* woB; const void* gw;
    void* out; char* ws;
};

__device__ __forceinline__ unsigned pack2(float a, float b) { f32x2 v = {a, b}; bf2_t r = __builtin_convertvector(v, bf2_t); return __builtin_bit_cast(unsigned, r); }
__device__ __forceinline__ void glds16(unsigned ldsaddr, unsigned voff, __amdgpu_buffer_rsrc_t rsrc, unsigned soff) {
    asm volatile("s_mov_b32 m0, %0\n\ts_nop 0\n\tbuffer_load_dwordx4 %1, %2, %3 offen lds" :: "s"(ldsaddr), "v"(voff), "s"(rsrc), "s"(soff) : "memory");
}
#define BAR() asm volatile("s_waitcnt lgkmcnt(0)\n\ts_barrier" ::: "memory")
#define WAITV(n) asm volatile("s_waitcnt vmcnt(" #n ")" ::: "memory")
__device__ __forceinline__ bf16x4 trread(const LAS char* p) { typedef short v4s __attribute__((ext_vector_type(4))); return __builtin_amdgcn_ds_read_tr16_b64_v4i16((LAS v4s*)p); }
__device__ __forceinline__ bf16x8 cat(bf16x4 a, bf16x4 b) { return __builtin_shufflevector(a, b, 0, 1, 2, 3, 4, 5, 6, 7); }
__device__ __forceinline__ f32x4 mfma16(bf16x8 a, bf16x8 b, f32x4 c) { return __builtin_amdgcn_mfma_f32_16x16x32_bf16(a, b, c, 0, 0, 0); }
__device__ __forceinline__ f32x16 mfma32(bf16x8 a, bf16x8 b, f32x16 c) { return __builtin_amdgcn_mfma_f32_32x32x16_bf16(a, b, c, 0, 0, 0); }
__device__ __forceinline__ f32x16 mfma32k8(bf16x4 a, bf16x4 b, f32x16 c) { return __builtin_amdgcn_mfma_f32_32x32x8bf16_1k(a, b, c, 0, 0, 0); }
__device__ __forceinline__ float swap_lo(float v) {
    auto r = __builtin_amdgcn_permlane32_swap(__float_as_uint(v), __float_as_uint(v), false, false); return __uint_as_float(r[0]); }
__device__ __forceinline__ float swap_max(float v) { auto r = __builtin_amdgcn_permlane32_swap(__float_as_uint(v), __float_as_uint(v), false, false); return fmaxf(__uint_as_float(r[0]), __uint_as_float(r[1])); }
__device__ __forceinline__ float swap_min(float v) { auto r = __builtin_amdgcn_permlane32_swap(__float_as_uint(v), __float_as_uint(v), false, false); return fminf(__uint_as_float(r[0]), __uint_as_float(r[1])); }
__device__ __forceinline__ float swap_sum(float v) { auto r = __builtin_amdgcn_permlane32_swap(__float_as_uint(v), __float_as_uint(v), false, false); return __uint_as_float(r[0]) + __uint_as_float(r[1]); }

__device__ __forceinline__ bool wait_flags(const char* p, bool active, u64 tag, char* ws) {
    for (unsigned spins = 0;;) {
        u64 v = active ? __hip_atomic_load((const GAS u64*)p, __ATOMIC_RELAXED, __HIP_MEMORY_SCOPE_AGENT) : tag;
        if (__all(v == tag)) return true;
        __builtin_amdgcn_s_sleep(1);
        if (++spins > SPIN_CAP) { __hip_atomic_store((GAS unsigned*)(ws + WS_TMO), 1u, __ATOMIC_RELAXED, __HIP_MEMORY_SCOPE_AGENT); return false; }
    }
}
__device__ __forceinline__ void set_flag(char* p, u64 tag) { __hip_atomic_store((GAS u64*)p, tag, __ATOMIC_RELAXED, __HIP_MEMORY_SCOPE_AGENT); }

#define STAMP(i) do {} while (0)

template <int WN>
__device__ __forceinline__ void gemm_loop(LAS char* lds, int w, int lane, const unsigned (&vx)[2], const unsigned (&vw)[4],
                                          __amdgpu_buffer_rsrc_t rx, __amdgpu_buffer_rsrc_t rw, f32x4 (&acc)[2][7], f32x4 (&accg)[2]) {
    const int wm = w & 3, fg = lane >> 4, l15 = lane & 15;
    const unsigned sw = (l15 >> 1) & 7;
    const unsigned xb0 = (32 * wm + l15) * 128 + ((fg ^ sw) << 4), xb1 = xb0 ^ 64;
    const unsigned wb = XS + (8 * fg + 2 * (l15 >> 2)) * 432 + 8 * (lane & 3) + WN * 7 * 32;
    const unsigned gb = L_GW + 16 * fg;
    const unsigned lbase = (unsigned)(size_t)lds;
    const bool six = w < 3;
    unsigned cur = 0, nxt2 = 2 * BUFS;
#pragma unroll 1
    for (int s = 0; s < 9; ++s) {
        if (s < 8) { if (six) WAITV(6); else WAITV(5); } else WAITV(0);
        BAR();
        if (s + 2 < 9) {
            const unsigned sx = (s + 2) * 128, sW = (s + 2) * (64 * 3456), lb = lbase + nxt2 + w * 1024;
            glds16(lb, vx[0], rx, sx); glds16(lb + 8192, vx[1], rx, sx);
            glds16(lb + 16384, vw[0], rw, sW); glds16(lb + 24576, vw[1], rw, sW); glds16(lb + 32768, vw[2], rw, sW);
            if (six) glds16(lb + 40960, vw[3], rw, sW);
        }
        const LAS char* xp0 = lds + cur + xb0; const LAS char* xp1 = lds + cur + xb1; const LAS char* wp = lds + cur + wb;
#pragma unroll
        for (int kk = 0; kk < 2; ++kk) {
            const LAS char* xp = kk ? xp1 : xp0;
            bf16x8 xf0 = *(const LAS bf16x8*)(xp), xf1 = *(const LAS bf16x8*)(xp + 2048);
            if (WN == 0) {
                bf16x8 gf = *(const LAS bf16x8*)(lds + gb + s * 128 + kk * 64);
                accg[0] = mfma16(gf, xf0, accg[0]); accg[1] = mfma16(gf, xf1, accg[1]);
            }
#pragma unroll
            for (int n = 0; n < 7; ++n) {
                bf16x8 wf = cat(trread(wp + kk * 13824 + n * 32), trread(wp + kk * 13824 + n * 32 + 432));
                if (WN * 7 + n <= 8) { acc[0][n] = mfma16(wf, xf0, acc[0][n]); acc[1][n] = mfma16(wf, xf1, acc[1][n]); }
                else                 { acc[0][n] = mfma16(xf0, wf, acc[0][n]); acc[1][n] = mfma16(xf1, wf, acc[1][n]); }
            }
        }
        cur = (cur == 2 * BUFS) ? 0 : cur + BUFS; nxt2 = (nxt2 == 2 * BUFS) ? 0 : nxt2 + BUFS;
    }
}

template <int WN>
__device__ __forceinline__ void write_images(LAS char* lds, int w, int lane, int br, const f32x4 (&acc)[2][7], const f32x4 (&accg)[2]) {
    const int wm = w & 3, fg = lane >> 4, l15 = lane & 15;
    const float qs = br ? QSCALE_B : LOG2E, ks = br ? 1.0f : LOG2E;
#pragma unroll
    for (int n = 0; n < 7; ++n) {
        const int T = WN * 7 + n;
        if (T <= 8) {
            const int f0 = 16 * T + 4 * fg;
            const int sec = f0 >= 72, ff = f0 - 72 * sec, hp = ff >= 36, m = (ff - 36 * hp) >> 2;
            const int hstride = sec ? 384 * 48 : 128 * 48;
            const int base = (sec ? L_K + 256 * 48 : L_Q) + 2 * hp * hstride + (32 * wm + l15) * 48;
            const int a0 = base + (m < 4 ? 8 * m : (m == 4 ? 32 : hstride + 8 * (m - 5)));
            const int a1 = (m == 4) ? base + hstride + 32 : a0 + 4;
            const float sc = sec ? ks : qs;
#pragma unroll
            for (int tt = 0; tt < 2; ++tt) {
                *(LAS unsigned*)(lds + a0 + tt * 768) = pack2(acc[tt][n][0] * sc, acc[tt][n][1] * sc);
                *(LAS unsigned*)(lds + a1 + tt * 768) = pack2(acc[tt][n][2] * sc, acc[tt][n][3] * sc);
            }
        } else {
            const int vf = 16 * (T - 9) + l15;
            const int pg = ((fg & 1) << 1) | (fg >> 1);
            const int a = L_V + vf * VROW + (256 + 32 * wm + 4 * pg) * 2;
            if (vf < 72) {
#pragma unroll
                for (int tt = 0; tt < 2; ++tt) {
                    u32x2 v = {pack2(acc[tt][n][0], acc[tt][n][1]), pack2(acc[tt][n][2], acc[tt][n][3])};
                    *(LAS u32x2*)(lds + a + tt * 32) = v;
                }
            }
        }
    }
    if (WN == 0 && lane < 16) {
#pragma unroll
        for (int tt = 0; tt < 2; ++tt) {
            const float z = accg[tt][0];
            *(LAS float*)(lds + L_G + (32 * wm + 16 * tt + l15) * 4) = 1.0f / (1.0f + __builtin_amdgcn_exp2f(-z * LOG2E));
        }
    }
}

__device__ __forceinline__ void write_o(LAS char* ob, int lane, int head, int tok0, const f32x16& o, float scale) {
    const int h = lane >> 5, tok = tok0 + (lane & 31);
    LAS char* a = ob + tok * 144 + head * 36 + 8 * h;
    *(LAS unsigned*)(a) = pack2(o[0] * scale, o[1] * scale);
    *(LAS unsigned*)(a + 4) = pack2(o[2] * scale, o[3] * scale);
    *(LAS unsigned*)(a + 16) = pack2(o[4] * scale, o[5] * scale);
    *(LAS unsigned*)(a + 20) = pack2(o[6] * scale, o[7] * scale);
    if (h == 0) *(LAS unsigned*)(a + 32) = pack2(o[8] * scale, o[9] * scale);
}

template <bool USEMAX>
__device__ __forceinline__ f32x16 attn_item(const LAS char* kp, const LAS char* vp, bf16x8 q0, bf16x4 q1, int kb0, int kbd, bool tail, int lane) {
    const int r = lane & 31, h = lane >> 5;
    float m = 0.f;
    if (USEMAX) {
        m = NEG;
        for (int kb = kb0; kb <= kbd; ++kb) {
            bf16x8 k0 = *(const LAS bf16x8*)(kp + kb * 1536);
            bf16x4 k1 = {0, 0, 0, 0}; { unsigned t = *(const LAS unsigned*)(kp + kb * 1536 + 32 - 16 * h); k1[0] = (short)(t & 0xffff); k1[1] = (short)(t >> 16); }
            f32x16 s = {}; s = mfma32(k0, q0, s); s = mfma32k8(k1, q1, s);
#pragma unroll
            for (int i = 0; i < 16; ++i) { const int c = (i & 3) + 8 * (i >> 2) + 4 * h;
                const bool ok = (kb == kbd) ? (c <= r) : ((tail && kb == kb0) ? (c > r) : true); m = fmaxf(m, ok ? s[i] : NEG); }
        }
        m = swap_max(m);
    }
    f32x16 o = {};
    for (int kb = kb0; kb <= kbd; ++kb) {
        bf16x8 k0 = *(const LAS bf16x8*)(kp + kb * 1536);
        bf16x4 k1 = {0, 0, 0, 0}; { unsigned t = *(const LAS unsigned*)(kp + kb * 1536 + 32 - 16 * h); k1[0] = (short)(t & 0xffff); k1[1] = (short)(t >> 16); }
        f32x16 s;
#pragma unroll
        for (int i = 0; i < 16; ++i) s[i] = -m;
        if (kb == kbd) {
#pragma unroll
            for (int i = 0; i < 16; ++i) { const int c = (i & 3) + 8 * (i >> 2) + 4 * h; s[i] = (c <= r) ? -m : NEG; }
        } else if (tail && kb == kb0) {
#pragma unroll
            for (int i = 0; i < 16; ++i) { const int c = (i & 3) + 8 * (i >> 2) + 4 * h; s[i] = (c > r) ? -m : NEG; }
        }
        s = mfma32(k0, q0, s); s = mfma32k8(k1, q1, s);
        u32x4 p0, p1;
#pragma unroll
        for (int i = 0; i < 4; ++i) {
            p0[i] = pack2(__builtin_amdgcn_exp2f(s[2 * i]), __builtin_amdgcn_exp2f(s[2 * i + 1]));
            p1[i] = pack2(__builtin_amdgcn_exp2f(s[8 + 2 * i]), __builtin_amdgcn_exp2f(s[9 + 2 * i]));
        }
        bf16x8 v0 = *(const LAS bf16x8*)(vp + kb * 64), v1 = *(const LAS bf16x8*)(vp + kb * 64 + 32);
        o = mfma32(v0, __builtin_bit_cast(bf16x8, p0), o);
        o = mfma32(v1, __builtin_bit_cast(bf16x8, p1), o);
    }
    return o;
}

__device__ __forceinline__ void features(const LAS char* rowp, int lane, bf16x8 wf0, bf16x4 wf1, unsigned (&pk)[9]) {
    const int h = lane >> 5;
    bf16x8 b0 = *(const LAS bf16x8*)(rowp + 16 * h);
    unsigned t = *(const LAS unsigned*)(rowp + 32); if (h) t = 0;
    bf16x4 b1 = {(short)(t & 0xffff), (short)(t >> 16), 0, 0};
    f32x16 d = {}; d = mfma32(wf0, b0, d); d = mfma32k8(wf1, b1, d);
    float mx = d[0], mn = d[0];
#pragma unroll
    for (int i = 1; i < 9; ++i) { mx = fmaxf(mx, d[i]); mn = fminf(mn, d[i]); }
    mx = swap_max(mx); mn = swap_min(mn);
    float ep[9], en[9], sp = 0.f, sn = 0.f;
#pragma unroll
    for (int i = 0; i < 9; ++i) { ep[i] = __builtin_amdgcn_exp2f(d[i] - mx); en[i] = __builtin_amdgcn_exp2f(mn - d[i]); sp += ep[i]; sn += en[i]; }
    sp = __builtin_amdgcn_rcpf(swap_sum(sp)); sn = __builtin_amdgcn_rcpf(swap_sum(sn));
    float v[18];
#pragma unroll
    for (int i = 0; i < 9; ++i) { v[i] = ep[i] * sp; v[9 + i] = en[i] * sn; }
#pragma unroll
    for (int i = 0; i < 9; ++i) pk[i] = pack2(v[2 * i], v[2 * i + 1]);
}

__global__ __launch_bounds__(512, 2) void k_phase12(Params p) {
    extern __shared__ __attribute__((aligned(16))) char smem[];
    LAS char* lds = (LAS char*)smem;
    const int tid = threadIdx.x, lane = tid & 63, w = __builtin_amdgcn_readfirstlane(tid >> 6);
    const int bid = blockIdx.x, g = bid & 7, idx = bid >> 3, br = idx & 1, rb = idx >> 1, r0 = rb * 128;
    STAMP(0);
    const __amdgpu_buffer_rsrc_t rx = __builtin_amdgcn_make_buffer_rsrc((void*)p.x, 0, S * D * 2, 0x00020000);
    const __amdgpu_buffer_rsrc_t rw = __builtin_amdgcn_make_buffer_rsrc((void*)(br ? p.wqkvB : p.wqkvA), 0, D * 3 * D * 2, 0x00020000);
    const unsigned lbase = (unsigned)(size_t)lds;
    unsigned vx[2], vw[4];
#pragma unroll
    for (int j = 0; j < 2; ++j) { const int row = 8 * (w + 8 * j) + (lane >> 3); vx[j] = (r0 + row) * 1152 + (((lane & 7) ^ ((row >> 1) & 7)) << 4); }
#pragma unroll
    for (int j = 0; j < 4; ++j) {
        const int ci = 64 * (w + 8 * j) + lane, rho = ci / 27, rem = ci - 27 * rho, sec = rem / 9, jj = rem - 9 * sec;
        const int k = (rho & ~7) + ((rho & 1) << 2) + ((rho >> 1) & 3);
        vw[j] = k * 3456 + sec * 1152 + g * 144 + jj * 16;
    }
    if (w == 3 || w == 4) { const __amdgpu_buffer_rsrc_t rg = __builtin_amdgcn_make_buffer_rsrc((void*)p.gw, 0, D * 2, 0x00020000);
        glds16(lbase + L_GW + (w - 3) * 1024, lane * 16, rg, (w - 3) * 1024); }
    if (w >= 5) { const __amdgpu_buffer_rsrc_t rp = __builtin_amdgcn_make_buffer_rsrc((void*)p.wphi, 0, 32 * 324 * 2, 0x00020000);
        glds16(lbase + L_WPHI + (w - 5) * 1024, lane * 16, rp, g * 2592 + (w - 5) * 1024); }
#pragma unroll
    for (int s = 0; s < 2; ++s) {
        const unsigned lb = lbase + s * BUFS + w * 1024, sx = s * 128, sW = s * (64 * 3456);
        glds16(lb, vx[0], rx, sx); glds16(lb + 8192, vx[1], rx, sx);
        glds16(lb + 16384, vw[0], rw, sW); glds16(lb + 24576, vw[1], rw, sW); glds16(lb + 32768, vw[2], rw, sW);
        if (w < 3) glds16(lb + 40960, vw[3], rw, sW);
    }
    const u64 tag = *(const __attribute__((address_space(4))) u64*)(p.ws + WS_EPOCH) + 1;
    asm volatile("" :: "s"(tag));
    f32x4 acc[2][7], accg[2];
#pragma unroll
    for (int t = 0; t < 2; ++t) { accg[t] = f32x4{0, 0, 0, 0};
#pragma unroll
        for (int n = 0; n < 7; ++n) acc[t][n] = f32x4{0, 0, 0, 0}; }
    if (w < 4) gemm_loop<0>(lds, w, lane, vx, vw, rx, rw, acc, accg); else gemm_loop<1>(lds, w, lane, vx, vw, rx, rw, acc, accg);
    STAMP(1);
    BAR();
    if (w < 4) write_images<0>(lds, w, lane, br, acc, accg); else write_images<1>(lds, w, lane, br, acc, accg);
    if (tid < 49) *(LAS u32x4*)(lds + L_V + 72 * VROW + tid * 16) = u32x4{0x3F803F80u, 0x3F803F80u, 0x3F803F80u, 0x3F803F80u};
    BAR();
    STAMP(2);
    const int head = w & 3, h = lane >> 5, r31 = lane & 31;
    const __amdgpu_buffer_rsrc_t rws = __builtin_amdgcn_make_buffer_rsrc((void*)p.ws, 0, 16 << 20, 0x00020000);
    LAS char* ob = lds + L_Q;
    if (br) {
        const unsigned kg = (unsigned)WS_KG + (rb * 8 + g) * KG_BYTES;
        if (rb < 15) {
#pragma unroll
            for (int j = 0; j < 3; ++j) { const int c = tid + 512 * j, hd = c / 384, wi = c - 384 * hd;
                u32x4 v = *(const LAS u32x4*)(lds + L_K + hd * 18432 + 12288 + wi * 16);
                __builtin_amdgcn_raw_buffer_store_b128(v, rws, kg + c * 16, 0, 16); }
#pragma unroll
            for (int j = 0; j < 3; ++j) { const int c = tid + 512 * j;
                if (c < 1152) { u32x4 v = *(const LAS u32x4*)(lds + L_V + (c >> 4) * VROW + 512 + (c & 15) * 16);
                    __builtin_amdgcn_raw_buffer_store_b128(v, rws, kg + KG_K + c * 16, 0, 16); } }
        }
        bf16x8 q0[2]; bf16x4 q1[2];
#pragma unroll
        for (int it = 0; it < 2; ++it) { const int j = (w >> 2) + 2 * it; const LAS char* qp = lds + L_Q + head * 6144 + (32 * j + r31) * 48;
            q0[it] = *(const LAS bf16x8*)(qp + 16 * h); unsigned t = *(const LAS unsigned*)(qp + 32); if (h) t = 0;
            q1[it] = bf16x4{(short)(t & 0xffff), (short)(t >> 16), 0, 0}; }
        WAITV(0);
        BAR();
        if (tid == 0) set_flag(p.ws + WS_FKV + (rb * 8 + g) * 128, tag);
        STAMP(3);
        if (rb > 0) {
            if (w == 0) wait_flags(p.ws + WS_FKV + ((rb - 1 - (lane & 1)) * 8 + g) * 128, lane < 2 && rb - 1 - lane >= 0, tag, p.ws);
            BAR();
            STAMP(4);
            u32x4 v[12];
#pragma unroll
            for (int b = 0; b < 2; ++b) {
                const unsigned src = (unsigned)WS_KG + ((rb - 1 - b) * 8 + g) * KG_BYTES;
                const bool ok = rb - 1 - b >= 0;
#pragma unroll
                for (int j = 0; j < 6; ++j) { const int c = tid + 512 * j;
                    v[b * 6 + j] = __builtin_amdgcn_raw_buffer_load_b128(rws, (ok && c < 2688) ? src + c * 16 : 0xFFFFFF00u, 0, 16); }
            }
#pragma unroll
            for (int b = 0; b < 2; ++b) {
                const bool ok = rb - 1 - b >= 0;
#pragma unroll
                for (int j = 0; j < 6; ++j) { const int c = tid + 512 * j;
                    if (ok && c < 2688) { int a;
                        if (c < 1536) { const int hd = c / 384, wi = c - 384 * hd; a = L_K + hd * 18432 + (1 - b) * 6144 + wi * 16; }
                        else { const int cc = c - 1536; a = L_V + (cc >> 4) * VROW + (1 - b) * 256 + (cc & 15) * 16; }
                        *(LAS u32x4*)(lds + a) = v[b * 6 + j]; } }
            }
            BAR();
        }
        STAMP(5);
        const LAS char* kp = lds + L_K + head * 18432 + r31 * 48 + 16 * h;
        const int vrow = r31 < 18 ? head * 18 + r31 : 72;
        const LAS char* vp = lds + L_V + vrow * VROW + 16 * h;
#pragma unroll
        for (int it = 0; it < 2; ++it) {
            const int j = (w >> 2) + 2 * it, J = 4 * rb + j;
            const int nb = J < 8 ? J : 8, kbd = 8 + j, kb0 = kbd - nb; const bool tail = J >= 8;
            f32x16 o = attn_item<false>(kp, vp, q0[it], q1[it], kb0, kbd, tail, lane);
            float l = swap_lo(o[10]);
            if (__any(!(l >= 9.3e-10f && l <= 1.07e9f))) {
                o = attn_item<true>(kp, vp, q0[it], q1[it], kb0, kbd, tail, lane);
                l = swap_lo(o[10]);
            }
            const float gt = *(const LAS float*)(lds + L_G + (32 * j + r31) * 4);
            write_o(ob, lane, head, 32 * j, o, (1.0f - gt) / l);
        }
    } else {
        const int th = w >> 2;
        bf16x8 wf0; bf16x4 wf1;
        { const int hh = (r31 >> 2) & 1, i = (r31 & 3) + 4 * (r31 >> 3); const bool valid = i <= 8 && r31 < 24; const int f = valid ? 9 * hh + i : 0;
          const int rot = (head & 1) ? 2 : 0; const LAS unsigned short* wp = (const LAS unsigned short*)(lds + L_WPHI) + head * 324 + f;
#pragma unroll
          for (int j = 0; j < 8; ++j) { unsigned short v = wp[(8 * h + j + rot) * 18]; wf0[j] = valid ? (short)v : (short)0; }
          const int d16 = (head & 1) ? 0 : 16;
          unsigned short a = wp[d16 * 18], b = wp[(d16 + 1) * 18];
          wf1 = (valid && h == 0) ? bf16x4{(short)a, (short)b, 0, 0} : bf16x4{0, 0, 0, 0}; }
        LAS char* pt = lds + L_K + (w >> 2) * 18432 + (w & 3) * 2560;
        f32x4 kv[3][2];
#pragma unroll
        for (int a = 0; a < 3; ++a) { kv[a][0] = f32x4{0, 0, 0, 0}; kv[a][1] = f32x4{0, 0, 0, 0}; }
        const int fg = lane >> 4, l15 = lane & 15;
        unsigned pq[2][9];
#pragma unroll
        for (int tb = 0; tb < 2; ++tb) {
            const int tok0 = 64 * th + 32 * tb;
            unsigned pk[9];
            features(lds + L_K + head * 18432 + (256 + tok0 + r31) * 48, lane, wf0, wf1, pk);
            LAS char* wr = pt + r31 * 80 + 40 * h;
            *(LAS u32x2*)(wr) = u32x2{pk[0], pk[1]}; *(LAS u32x2*)(wr + 8) = u32x2{pk[2], pk[3]}; *(LAS u32x2*)(wr + 16) = u32x2{pk[4], pk[5]};
            *(LAS u32x2*)(wr + 24) = u32x2{pk[6], pk[7]}; *(LAS unsigned*)(wr + 32) = pk[8];
            asm volatile("s_waitcnt lgkmcnt(0)" ::: "memory");
            const LAS char* tr = pt + (16 * (fg >> 1) + 4 * (fg & 1) + (l15 >> 2)) * 80 + 8 * (lane & 3);
            const int vr0 = head * 18 + l15, vr1 = l15 < 2 ? head * 18 + 16 + l15 : 72;
            bf16x8 vb0 = *(const LAS bf16x8*)(lds + L_V + vr0 * VROW + (256 + tok0 + 8 * fg) * 2);
            bf16x8 vb1 = *(const LAS bf16x8*)(lds + L_V + vr1 * VROW + (256 + tok0 + 8 * fg) * 2);
#pragma unroll
            for (int ft = 0; ft < 3; ++ft) {
                bf16x8 af = cat(trread(tr + ft * 32), trread(tr + ft * 32 + 8 * 80));
                kv[ft][0] = mfma16(af, vb0, kv[ft][0]); kv[ft][1] = mfma16(af, vb1, kv[ft][1]);
            }
            asm volatile("s_waitcnt lgkmcnt(0)" ::: "memory");
        }
        LAS char* kvf = lds + L_K + 2 * 18432;
        if (th == 0) {
#pragma unroll
            for (int ft = 0; ft < 3; ++ft)
#pragma unroll
                for (int dt = 0; dt < 2; ++dt) { const int drow = 16 * dt + l15, f = 16 * ft + 4 * fg;
                    if (drow < 19 && f < 40) *(LAS f32x4*)(kvf + (head * 19 + drow) * 160 + f * 4) = kv[ft][dt]; }
        }
        BAR();
        if (th == 1) {
#pragma unroll
            for (int ft = 0; ft < 3; ++ft)
#pragma unroll
                for (int dt = 0; dt < 2; ++dt) { const int drow = 16 * dt + l15, f = 16 * ft + 4 * fg;
                    if (drow < 19 && f < 40) { LAS f32x4* a = (LAS f32x4*)(kvf + (head * 19 + drow) * 160 + f * 4); *a = *a + kv[ft][dt]; } }
        }
        BAR();
        const unsigned kvp = (unsigned)WS_KVP + (g * 16 + rb) * KVP_BYTES;
#pragma unroll
        for (int j = 0; j < 2; ++j) { const int c = tid + 512 * j;
            if (c < KVP_CHUNKS) __builtin_amdgcn_raw_buffer_store_b128(*(const LAS u32x4*)(kvf + c * 16), rws, kvp + c * 16, 0, 16); }
#pragma unroll
        for (int tb = 0; tb < 2; ++tb) features(lds + L_Q + head * 6144 + (64 * th + 32 * tb + r31) * 48, lane, wf0, wf1, pq[tb]);
        WAITV(0);
        BAR();
        if (tid == 0) set_flag(p.ws + WS_FKP + (rb * 8 + g) * 128, tag);
        STAMP(3);
        if (w == 0) wait_flags(p.ws + WS_FKP + ((lane & 15) * 8 + g) * 128, lane < 16, tag, p.ws);
        BAR();
        STAMP(4);
        LAS char* kvb = lds + L_K + 3 * 18432;
#pragma unroll
        for (int j = 0; j < 2; ++j) { const int c = tid + 512 * j;
            if (c < KVP_CHUNKS) {
                f32x4 s = {0, 0, 0, 0};
#pragma unroll
                for (int i = 0; i < 16; ++i) { u32x4 v = __builtin_amdgcn_raw_buffer_load_b128(rws, (unsigned)WS_KVP + (g * 16 + i) * KVP_BYTES + c * 16, 0, 16);
                    s += __builtin_bit_cast(f32x4, v); }
                const int row = c / 10, f = (c - 10 * row) * 4;
                const int fo = f >= 20 ? 48 + (f - 20) * 2 : f * 2;
                *(LAS u32x2*)(kvb + row * 96 + fo) = u32x2{pack2(s[0], s[1]), pack2(s[2], s[3])};
            } }
        BAR();
        STAMP(5);
        const LAS char* ap = kvb + (head * 19 + (r31 < 19 ? r31 : 18)) * 96 + 48 * h;
        const bf16x8 a0 = *(const LAS bf16x8*)(ap), a1 = *(const LAS bf16x8*)(ap + 16);
        unsigned t2 = *(const LAS unsigned*)(ap + 32);
        const bf16x4 a2 = {(short)(t2 & 0xffff), (short)(t2 >> 16), 0, 0};
#pragma unroll
        for (int tb = 0; tb < 2; ++tb) {
            f32x16 o = {};
            o = mfma32(a0, __builtin_bit_cast(bf16x8, u32x4{pq[tb][0], pq[tb][1], pq[tb][2], pq[tb][3]}), o);
            o = mfma32(a1, __builtin_bit_cast(bf16x8, u32x4{pq[tb][4], pq[tb][5], pq[tb][6], pq[tb][7]}), o);
            o = mfma32k8(a2, __builtin_bit_cast(bf16x4, u32x2{pq[tb][8], 0u}), o);
            const int tok0 = 64 * th + 32 * tb;
            const float den = swap_lo(o[10]) + 1e-6f;
            const float gt = *(const LAS float*)(lds + L_G + (tok0 + r31) * 4);
            write_o(ob, lane, head, tok0, o, gt / den);
        }
    }
    BAR();
    STAMP(6);
    { const unsigned od = (unsigned)WS_O + ((br * 8 + g) * 2048 + r0) * 144;
#pragma unroll
      for (int j = 0; j < 3; ++j) { const int c = tid + 512 * j;
          if (c < 1152) __builtin_amdgcn_raw_buffer_store_b128(*(const LAS u32x4*)(ob + c * 16), rws, od + c * 16, 0, 16); } }
    if (bid == 0 && w == 7) {
        bool ok = true;
#pragma unroll
        for (int j = 0; j < 2; ++j) { ok &= wait_flags(p.ws + WS_FKV + (lane + 64 * j) * 128, true, tag, p.ws); ok &= wait_flags(p.ws + WS_FKP + (lane + 64 * j) * 128, true, tag, p.ws); }
        if (lane == 0) set_flag(p.ws + WS_EPOCH, tag);
    }
    STAMP(7);
}

constexpr int P3_OB = 0, P3_W0 = 46080, P3_WQ = 36864;
__global__ __launch_bounds__(512, 2) void k_phase3(Params p) {
    extern __shared__ __attribute__((aligned(16))) char smem[];
    LAS char* lds = (LAS char*)smem;
    const int tid = threadIdx.x, lane = tid & 63, w = __builtin_amdgcn_readfirstlane(tid >> 6);
    const int bid = blockIdx.x, ct = bid % 9, rt = bid / 9, t0 = rt * 80;
    const unsigned lbase = (unsigned)(size_t)lds;
    const __amdgpu_buffer_rsrc_t ro = __builtin_amdgcn_make_buffer_rsrc((void*)(p.ws + WS_O), 0, 16 * 2048 * 144, 0x00020000);
    const __amdgpu_buffer_rsrc_t rwa = __builtin_amdgcn_make_buffer_rsrc((void*)p.woA, 0, D * D * 2, 0x00020000);
    const __amdgpu_buffer_rsrc_t rwb = __builtin_amdgcn_make_buffer_rsrc((void*)p.woB, 0, D * D * 2, 0x00020000);
    const int wrow = lane >> 3, wf = ((wrow >> 1) & 1) | (((wrow >> 3) & 1) << 1);
    auto issue_w = [&](int q) {
        const unsigned lb = lbase + P3_W0 + (q & 1) * P3_WQ;
#pragma unroll
        for (int j = 0; j < 5; ++j) { const int u = w + 8 * j;
            if (u < 36) { const int row = 8 * u + wrow, f = ((row >> 1) & 1) | (((row >> 3) & 1) << 1);
                const unsigned vo = row * 1152 + ct * 128 + (((lane & 7) ^ (f << 1)) << 4);
                glds16(lb + u * 1024, vo, q < 2 ? rwa : rwb, (q & 1) * 288 * 1152); } }
    };
    (void)wf;
    u32x4 ov[6];
    auto load_o = [&](int q) {
#pragma unroll
        for (int j = 0; j < 6; ++j) { const int c = tid + 512 * j, sl = c / 720, wi = c - 720 * sl;
            ov[j] = __builtin_amdgcn_raw_buffer_load_b128(ro, c < 2880 ? ((4 * q + sl) * 2048 + t0) * 144 + wi * 16 : 0xFFFFFF00u, 0, 16); }
    };
    load_o(0); issue_w(0);
    const int fg = lane >> 4, l15 = lane & 15;
    const int kh = w >> 2, cp = (w >> 1) & 1, tg = (w & 1) ^ kh;
    const int tt0 = tg ? 3 : 0;
    f32x4 acc[3][2];
#pragma unroll
    for (int a = 0; a < 3; ++a) { acc[a][0] = f32x4{0, 0, 0, 0}; acc[a][1] = f32x4{0, 0, 0, 0}; }
    const int fl = ((l15 >> 3) & 1) | ((fg & 1) << 1);
    const unsigned wl0 = (8 * fg + (l15 >> 2)) * 128 + (((2 * cp) ^ fl) << 5) + 8 * (lane & 3), wl1 = (8 * fg + (l15 >> 2)) * 128 + (((2 * cp + 1) ^ fl) << 5) + 8 * (lane & 3);
    for (int q = 0; q < 4; ++q) {
        BAR();
#pragma unroll
        for (int j = 0; j < 6; ++j) { const int c = tid + 512 * j; if (c < 2880) *(LAS u32x4*)(lds + P3_OB + c * 16) = ov[j]; }
        WAITV(0);
        BAR();
        if (q < 3) { load_o(q + 1); issue_w(q + 1); }
        const LAS char* wq = lds + P3_W0 + (q & 1) * P3_WQ;
        for (int ks = (kh ^ (q & 1)); ks < 9; ks += 2) {
            const int ci = 4 * ks + fg, sl = ci / 9, jj = ci - 9 * sl;
            const LAS char* op = lds + P3_OB + sl * 11520 + (16 * tt0 + l15) * 144 + jj * 16;
            bf16x8 w0 = cat(trread(wq + wl0 + ks * 4096), trread(wq + wl0 + ks * 4096 + 512));
            bf16x8 w1 = cat(trread(wq + wl1 + ks * 4096), trread(wq + wl1 + ks * 4096 + 512));
#pragma unroll
            for (int a = 0; a < 3; ++a) if (a < 2 || tg == 0) {
                bf16x8 of = *(const LAS bf16x8*)(op + a * 16 * 144);
                acc[a][0] = mfma16(w0, of, acc[a][0]); acc[a][1] = mfma16(w1, of, acc[a][1]);
            }
        }
    }
    BAR();
    LAS char* red = lds; LAS char* ot = lds + 24576;
    if (kh == 1) {
#pragma unroll
        for (int a = 0; a < 3; ++a) if (a < 2 || tg == 0)
#pragma unroll
            for (int c = 0; c < 2; ++c) *(LAS f32x4*)(red + (16 * (tt0 + a) + l15) * 272 + (32 * cp + 16 * c + 4 * fg) * 4) = acc[a][c];
    }
    BAR();
    if (kh == 0) {
#pragma unroll
        for (int a = 0; a < 3; ++a) if (a < 2 || tg == 0)
#pragma unroll
            for (int c = 0; c < 2; ++c) { f32x4 v = acc[a][c] + *(const LAS f32x4*)(red + (16 * (tt0 + a) + l15) * 272 + (32 * cp + 16 * c + 4 * fg) * 4);
                *(LAS u32x2*)(ot + (16 * (tt0 + a) + l15) * 128 + (32 * cp + 16 * c + 4 * fg) * 2) = u32x2{pack2(v[0], v[1]), pack2(v[2], v[3])}; }
    }
    BAR();
#pragma unroll
    for (int j = 0; j < 2; ++j) { const int c = tid + 512 * j, row = c >> 3;
        if (c < 640 && t0 + row < S) *(u32x4*)((char*)p.out + (size_t)(t0 + row) * 1152 + ct * 128 + (c & 7) * 16) = *(const LAS u32x4*)(ot + c * 16); }
}

extern "C" void kernel_launch(void* const* d_in, const int* in_sizes, int n_in, void* d_out, int out_size, void* d_ws, size_t ws_size, hipStream_t stream) {
    Params p;
    p.x = d_in[0]; p.wqkvA = d_in[1]; p.wphi = d_in[2]; p.woA = d_in[3]; p.wqkvB = d_in[5]; p.woB = d_in[6]; p.gw = d_in[7];
    p.out = d_out; p.ws = (char*)d_ws;
    hipFuncSetAttribute((const void*)k_phase12, hipFuncAttributeMaxDynamicSharedMemorySize, LDS_BYTES);
    hipFuncSetAttribute((const void*)k_phase3, hipFuncAttributeMaxDynamicSharedMemorySize, 120 * 1024);
    k_phase12<<<256, 512, LDS_BYTES, stream>>>(p);
    k_phase3<<<234, 512, 120 * 1024, stream>>>(p);
}
```

```cpp
#include <hip/hip_runtime.h>
#include <stdint.h>

#define LAS __attribute__((address_space(3)))
#define GAS __attribute__((address_space(1)))
typedef short bf16x8 __attribute__((ext_vector_type(8)));
typedef short bf16x4 __attribute__((ext_vector_type(4)));
typedef float f32x2 __attribute__((ext_vector_type(2)));
typedef float f32x4 __attribute__((ext_vector_type(4)));
typedef float f32x16 __attribute__((ext_vector_type(16)));
typedef unsigned u32x2 __attribute__((ext_vector_type(2)));
typedef unsigned u32x4 __attribute__((ext_vector_type(4)));
typedef __bf16 bf2_t __attribute__((ext_vector_type(2)));
typedef unsigned long long u64;

constexpr int S = 2048, D = 576;
constexpr float LOG2E = 1.4426950408889634f;
constexpr float QSCALE_B = 0.23570226039551584f * LOG2E;
constexpr float NEG = -1.0e30f;

constexpr size_t WS_EPOCH = 0, WS_FKV = 4096, WS_FKP = 20480, WS_FO = 36864, WS_TMO = 69632;
constexpr size_t WS_KG = 131072;
constexpr int KG_BYTES = 43008, KG_K = 24576;
constexpr size_t WS_KVP = 6291456;
constexpr int KVP_BYTES = 12288, KVP_CHUNKS = 760;
constexpr size_t WS_O = 8388608;
constexpr size_t WS_STAMP = 16777216;

constexpr int XS = 16384, WSB = 27648, BUFS = XS + WSB;
constexpr int L_K = 0;
constexpr int L_V = 73728, VROW = 784;
constexpr int L_Q = 130960;
constexpr int L_G = 155648;
constexpr int L_GW = 158720;
constexpr int L_WPHI = 160768;
constexpr int LDS_BYTES = 163840;
constexpr unsigned SPIN_CAP = 1u << 18;

struct Params {
    const void* x; const void* wqkvA; const void* wphi; const void* woA; const void* wqkvB; const void* woB; const void* gw;
    void* out; char* ws;
};

__device__ __forceinline__ unsigned pack2(float a, float b) { f32x2 v = {a, b}; bf2_t r = __builtin_convertvector(v, bf2_t); return __builtin_bit_cast(unsigned, r); }
__device__ __forceinline__ void glds16(unsigned ldsaddr, unsigned voff, __amdgpu_buffer_rsrc_t rsrc, unsigned soff) {
    asm volatile("s_mov_b32 m0, %0\n\ts_nop 0\n\tbuffer_load_dwordx4 %1, %2, %3 offen lds" :: "s"(ldsaddr), "v"(voff), "s"(rsrc), "s"(soff) : "memory");
}
#define BAR() asm volatile("s_waitcnt lgkmcnt(0)\n\ts_barrier" ::: "memory")
#define WAITV(n) asm volatile("s_waitcnt vmcnt(" #n ")" ::: "memory")
__device__ __forceinline__ bf16x4 trread(const LAS char* p) { typedef short v4s __attribute__((ext_vector_type(4))); return __builtin_amdgcn_ds_read_tr16_b64_v4i16((LAS v4s*)p); }
__device__ __forceinline__ bf16x8 cat(bf16x4 a, bf16x4 b) { return __builtin_shufflevector(a, b, 0, 1, 2, 3, 4, 5, 6, 7); }
__device__ __forceinline__ f32x4 mfma16(bf16x8 a, bf16x8 b, f32x4 c) { return __builtin_amdgcn_mfma_f32_16x16x32_bf16(a, b, c, 0, 0, 0); }
__device__ __forceinline__ f32x16 mfma32(bf16x8 a, bf16x8 b, f32x16 c) { return __builtin_amdgcn_mfma_f32_32x32x16_bf16(a, b, c, 0, 0, 0); }
__device__ __forceinline__ f32x16 mfma32k8(bf16x4 a, bf16x4 b, f32x16 c) { return __builtin_amdgcn_mfma_f32_32x32x8bf16_1k(a, b, c, 0, 0, 0); }
__device__ __forceinline__ float swap_lo(float v) {
    auto r = __builtin_amdgcn_permlane32_swap(__float_as_uint(v), __float_as_uint(v), false, false); return __uint_as_float(r[0]); }
__device__ __forceinline__ float swap_max(float v) { auto r = __builtin_amdgcn_permlane32_swap(__float_as_uint(v), __float_as_uint(v), false, false); return fmaxf(__uint_as_float(r[0]), __uint_as_float(r[1])); }
__device__ __forceinline__ float swap_min(float v) { auto r = __builtin_amdgcn_permlane32_swap(__float_as_uint(v), __float_as_uint(v), false, false); return fminf(__uint_as_float(r[0]), __uint_as_float(r[1])); }
__device__ __forceinline__ float swap_sum(float v) { auto r = __builtin_amdgcn_permlane32_swap(__float_as_uint(v), __float_as_uint(v), false, false); return __uint_as_float(r[0]) + __uint_as_float(r[1]); }

__device__ __forceinline__ bool wait_flags(const char* p, bool active, u64 tag, char* ws) {
    for (unsigned spins = 0;;) {
        u64 v = active ? __hip_atomic_load((const GAS u64*)p, __ATOMIC_RELAXED, __HIP_MEMORY_SCOPE_AGENT) : tag;
        if (__all(v == tag)) return true;
        __builtin_amdgcn_s_sleep(1);
        if (++spins > SPIN_CAP) { __hip_atomic_store((GAS unsigned*)(ws + WS_TMO), 1u, __ATOMIC_RELAXED, __HIP_MEMORY_SCOPE_AGENT); return false; }
    }
}
__device__ __forceinline__ void set_flag(char* p, u64 tag) { __hip_atomic_store((GAS u64*)p, tag, __ATOMIC_RELAXED, __HIP_MEMORY_SCOPE_AGENT); }

#define STAMP(i) do {} while (0)
#define STAMPW(i, ws_) do {} while (0)

template <int V> struct WT { static constexpr int T0 = V == 0 ? 0 : V == 1 ? 4 : V == 2 ? 7 : 11, NC = (V & 1) ? 3 : 4; };
constexpr int WSEC = 64 * 144;
template <int V> struct Frags { bf16x8 x[4], w[WT<V>::NC]; };
template <int V>
__device__ __forceinline__ void read_frags(Frags<V>& f, const LAS char* xp, const LAS char* wp, const LAS char* wp4, const LAS char* gp, bool gsel) {
#pragma unroll
    for (int t = 0; t < 4; ++t) f.x[t] = *(const LAS bf16x8*)(xp + t * 2048);
#pragma unroll
    for (int n = 0; n < WT<V>::NC; ++n) {
        constexpr int T0 = WT<V>::T0; const int T = T0 + n, sec = (16 * T) / 72, off = sec * WSEC + (16 * T - 72 * sec) * 2;
        if (T == 4) f.w[n] = cat(trread(wp4), trread(wp4 + 144));
        else f.w[n] = cat(trread(wp + off), trread(wp + off + 144));
    }
    if (V == 3) { const bf16x8 gf = *(const LAS bf16x8*)(gp); const u32x4 a = __builtin_bit_cast(u32x4, f.w[2]), b = __builtin_bit_cast(u32x4, gf);
        f.w[2] = __builtin_bit_cast(bf16x8, u32x4{gsel ? b[0] : a[0], gsel ? b[1] : a[1], gsel ? b[2] : a[2], gsel ? b[3] : a[3]}); }
}
template <int V>
__device__ __forceinline__ void mma_frags(const Frags<V>& f, f32x4 (&acc)[4][4]) {
#pragma unroll
    for (int n = 0; n < WT<V>::NC; ++n)
#pragma unroll
        for (int t = 0; t < 4; ++t) acc[t][n] = (WT<V>::T0 + n <= 8) ? mfma16(f.w[n], f.x[t], acc[t][n]) : mfma16(f.x[t], f.w[n], acc[t][n]);
}
template <int V>
__device__ __forceinline__ void interleave() {
#define SG1 __builtin_amdgcn_sched_group_barrier(0x008, 1, 0); __builtin_amdgcn_sched_group_barrier(0x100, 1, 0);
    SG1 SG1 SG1 SG1 SG1 SG1 SG1 SG1 SG1 SG1
    if (V != 1) { SG1 }
    if (V == 0 || V == 2) { SG1 }
#undef SG1
}
template <int V>
__device__ __forceinline__ void gemm_loop(LAS char* lds, int w, int lane, f32x4 (&acc)[4][4]) {
    const int fg = lane >> 4, l15 = lane & 15, pp = lane & 3;
    const unsigned sw = (l15 >> 1) & 7;
    const unsigned xb0 = (64 * (w & 1) + l15) * 128 + ((fg ^ sw) << 4), xb1 = xb0 ^ 64;
    const unsigned wb = XS + (8 * fg + 2 * (l15 >> 2)) * 144 + 8 * pp;
    const unsigned wb4 = wb + (pp < 2 ? 128 : WSEC - 16);
    const LAS char* gp = lds + L_GW + 16 * fg;
    const bool gsel = l15 >= 8;
    unsigned cur = 0;
    Frags<V> f0, f1;
    BAR();
    read_frags<V>(f0, lds + xb0, lds + wb, lds + wb4, gp, gsel);
#pragma unroll 1
    for (int s = 0; s < 9; ++s) {
        read_frags<V>(f1, lds + cur + xb1, lds + cur + wb + 32 * 144, lds + cur + wb4 + 32 * 144, gp + 64, gsel);
        mma_frags<V>(f0, acc);
        interleave<V>();
        __builtin_amdgcn_sched_barrier(0);
        cur = (cur == 2 * BUFS) ? 0 : cur + BUFS; gp += 128;
        if (s < 8) BAR();
        read_frags<V>(f0, lds + cur + xb0, lds + cur + wb, lds + cur + wb4, gp, gsel);
        mma_frags<V>(f1, acc);
        interleave<V>();
        __builtin_amdgcn_sched_barrier(0);
    }
}

template <int LW>
__device__ __forceinline__ u64 load_loop(const Params& p, LAS char* lds, int lane, int g, int br, int r0) {
    constexpr int NV = LW ? 4 : 5;
    const __amdgpu_buffer_rsrc_t rx = __builtin_amdgcn_make_buffer_rsrc((void*)p.x, 0, S * D * 2, 0x00020000);
    const __amdgpu_buffer_rsrc_t rw = __builtin_amdgcn_make_buffer_rsrc((void*)(br ? p.wqkvB : p.wqkvA), 0, D * 3 * D * 2, 0x00020000);
    const unsigned lbase = (unsigned)(size_t)lds;
    const unsigned vx = (r0 + 8 * LW + (lane >> 3)) * 1152 + (((lane & 7) ^ ((4 * LW + (lane >> 4)) & 7)) << 4);
    auto issue_x = [&](int s, unsigned buf) {
#pragma unroll
        for (int i = 0; i < 8; ++i) glds16(lbase + buf + LW * 1024 + i * 2048, vx, rx, s * 128 + i * (16 * 1152));
    };
    issue_x(0, 0);
    unsigned vw[NV];
#pragma unroll
    for (int j = 0; j < NV; ++j) {
        const int ci = 64 * (LW + 2 * j) + lane, rho = (ci * 7282) >> 16, jj = ci - 9 * rho;
        const int k = (rho & ~7) + ((rho & 1) << 2) + ((rho >> 1) & 3);
        vw[j] = k * 3456 + g * 144 + jj * 16;
    }
    auto issue_w = [&](int s, unsigned buf) {
#pragma unroll
        for (int sec = 0; sec < 3; ++sec)
#pragma unroll
            for (int j = 0; j < NV; ++j) glds16(lbase + buf + XS + sec * WSEC + (LW + 2 * j) * 1024, vw[j], rw, s * (64 * 3456) + sec * 1152);
    };
    issue_w(0, 0);
    if (LW == 0) { const __amdgpu_buffer_rsrc_t rg = __builtin_amdgcn_make_buffer_rsrc((void*)p.gw, 0, D * 2, 0x00020000);
        glds16(lbase + L_GW, lane * 16, rg, 0); glds16(lbase + L_GW + 1024, lane * 16, rg, 1024); }
    else if (!br) { const __amdgpu_buffer_rsrc_t rp = __builtin_amdgcn_make_buffer_rsrc((void*)p.wphi, 0, 32 * 324 * 2, 0x00020000);
#pragma unroll
        for (int i = 0; i < 3; ++i) glds16(lbase + L_WPHI + i * 1024, lane * 16, rp, g * 2592 + i * 1024); }
    issue_x(1, BUFS); issue_w(1, BUFS);
    const u64 tag = *(const __attribute__((address_space(4))) u64*)(p.ws + WS_EPOCH) + 1;
    asm volatile("" :: "s"(tag));
    unsigned nxt2 = 2 * BUFS;
#pragma unroll 1
    for (int s = 0; s < 9; ++s) {
        if (s < 8) { if (LW) WAITV(20); else WAITV(23); } else WAITV(0);
        BAR();
        if (s + 2 < 9) { issue_x(s + 2, nxt2); issue_w(s + 2, nxt2); }
        nxt2 = (nxt2 == 2 * BUFS) ? 0 : nxt2 + BUFS;
    }
    return tag;
}

template <int V>
__device__ __forceinline__ void image_addr(int w, int lane, unsigned (&a0)[4], unsigned (&a1)[4]) {
    const int fg = lane >> 4, l15 = lane & 15;
#pragma unroll
    for (int n = 0; n < 4; ++n) { a0[n] = 0; a1[n] = 0; }
#pragma unroll
    for (int n = 0; n < WT<V>::NC; ++n) {
        const int T = WT<V>::T0 + n;
        if (T <= 8) {
            const int G = 4 * T + fg, q4 = (G * 57) >> 9, m = G - 9 * q4, sec = q4 >> 1, hp = q4 & 1;
            const int hstride = sec ? 384 * 48 : 128 * 48;
            const int base = (sec ? L_K + 256 * 48 : L_Q) + 2 * hp * hstride + (64 * (w & 1) + l15) * 48;
            a0[n] = base + (m < 4 ? 8 * m : (m == 4 ? 32 : hstride + 8 * (m - 5)));
            a1[n] = (m == 4) ? base + hstride + 32 : a0[n] + 4;
            asm volatile("" : "+v"(a0[n])); if (T & 1) asm volatile("" : "+v"(a1[n]));
        }
    }
    if (V >= 2) { a0[3] = L_V + l15 * VROW + (256 + 64 * (w & 1) + 4 * (((fg & 1) << 1) | (fg >> 1))) * 2; asm volatile("" : "+v"(a0[3])); }
}
template <int V>
__device__ __forceinline__ void write_images(LAS char* lds, int w, int lane, int br, const unsigned (&a0)[4], const unsigned (&a1)[4], const f32x4 (&acc)[4][4]) {
    const int fg = lane >> 4, l15 = lane & 15;
    const float qs = br ? QSCALE_B : LOG2E, ks = br ? 1.0f : LOG2E;
#pragma unroll
    for (int n = 0; n < WT<V>::NC; ++n) {
        const int T = WT<V>::T0 + n;
        if (T <= 8) {
            const float sc = T < 4 ? qs : (T > 4 ? ks : (fg < 2 ? qs : ks));
#pragma unroll
            for (int tt = 0; tt < 4; ++tt) {
                const unsigned lo = pack2(acc[tt][n][0] * sc, acc[tt][n][1] * sc), hi = pack2(acc[tt][n][2] * sc, acc[tt][n][3] * sc);
                if (T & 1) { *(LAS unsigned*)(lds + a0[n] + tt * 768) = lo; *(LAS unsigned*)(lds + a1[n] + tt * 768) = hi; }
                else *(LAS u32x2*)(lds + a0[n] + tt * 768) = u32x2{lo, hi};
            }
        } else if (T < 13 || l15 < 8) {
#pragma unroll
            for (int tt = 0; tt < 4; ++tt)
                *(LAS u32x2*)(lds + a0[3] + (T - 9) * 16 * VROW + tt * 32) = u32x2{pack2(acc[tt][n][0], acc[tt][n][1]), pack2(acc[tt][n][2], acc[tt][n][3])};
        }
    }
    if (V == 3 && l15 == 8) {
#pragma unroll
        for (int tt = 0; tt < 4; ++tt) { f32x4 gv;
#pragma unroll
            for (int r = 0; r < 4; ++r) gv[r] = __builtin_amdgcn_rcpf(1.0f + __builtin_amdgcn_exp2f(-acc[tt][2][r] * LOG2E));
            *(LAS f32x4*)(lds + L_G + (64 * (w & 1) + 16 * tt + 4 * fg) * 4) = gv; }
    }
}

__device__ __forceinline__ void write_o(LAS char* ob, int lane, int head, int tok0, const f32x16& o, float scale) {
    const int h = lane >> 5, tok = tok0 + (lane & 31);
    LAS char* a = ob + tok * 144 + head * 36 + 8 * h;
    *(LAS unsigned*)(a) = pack2(o[0] * scale, o[1] * scale);
    *(LAS unsigned*)(a + 4) = pack2(o[2] * scale, o[3] * scale);
    *(LAS unsigned*)(a + 16) = pack2(o[4] * scale, o[5] * scale);
    *(LAS unsigned*)(a + 20) = pack2(o[6] * scale, o[7] * scale);
    if (h == 0) *(LAS unsigned*)(a + 32) = pack2(o[8] * scale, o[9] * scale);
}

template <bool USEMAX>
__device__ __forceinline__ f32x16 attn_item(const LAS char* kp, const LAS char* vp, bf16x8 q0, bf16x4 q1, int kb0, int kbd, bool tail, int lane) {
    const int r = lane & 31, h = lane >> 5;
    float m = 0.f;
    if (USEMAX) {
        m = NEG;
        for (int kb = kb0; kb <= kbd; ++kb) {
            bf16x8 k0 = *(const LAS bf16x8*)(kp + kb * 1536);
            bf16x4 k1 = {0, 0, 0, 0}; { unsigned t = *(const LAS unsigned*)(kp + kb * 1536 + 32 - 16 * h); k1[0] = (short)(t & 0xffff); k1[1] = (short)(t >> 16); }
            f32x16 s = {}; s = mfma32(k0, q0, s); s = mfma32k8(k1, q1, s);
#pragma unroll
            for (int i = 0; i < 16; ++i) { const int c = (i & 3) + 8 * (i >> 2) + 4 * h;
                const bool ok = (kb == kbd) ? (c <= r) : ((tail && kb == kb0) ? (c > r) : true); m = fmaxf(m, ok ? s[i] : NEG); }
        }
        m = swap_max(m);
    }
    f32x16 o = {};
    for (int kb = kb0; kb <= kbd; ++kb) {
        bf16x8 k0 = *(const LAS bf16x8*)(kp + kb * 1536);
        bf16x4 k1 = {0, 0, 0, 0}; { unsigned t = *(const LAS unsigned*)(kp + kb * 1536 + 32 - 16 * h); k1[0] = (short)(t & 0xffff); k1[1] = (short)(t >> 16); }
        f32x16 s;
#pragma unroll
        for (int i = 0; i < 16; ++i) s[i] = -m;
        if (kb == kbd) {
#pragma unroll
            for (int i = 0; i < 16; ++i) { const int c = (i & 3) + 8 * (i >> 2) + 4 * h; s[i] = (c <= r) ? -m : NEG; }
        } else if (tail && kb == kb0) {
#pragma unroll
            for (int i = 0; i < 16; ++i) { const int c = (i & 3) + 8 * (i >> 2) + 4 * h; s[i] = (c > r) ? -m : NEG; }
        }
        s = mfma32(k0, q0, s); s = mfma32k8(k1, q1, s);
        u32x4 p0, p1;
#pragma unroll
        for (int i = 0; i < 4; ++i) {
            p0[i] = pack2(__builtin_amdgcn_exp2f(s[2 * i]), __builtin_amdgcn_exp2f(s[2 * i + 1]));
            p1[i] = pack2(__builtin_amdgcn_exp2f(s[8 + 2 * i]), __builtin_amdgcn_exp2f(s[9 + 2 * i]));
        }
        bf16x8 v0 = *(const LAS bf16x8*)(vp + kb * 64), v1 = *(const LAS bf16x8*)(vp + kb * 64 + 32);
        o = mfma32(v0, __builtin_bit_cast(bf16x8, p0), o);
        o = mfma32(v1, __builtin_bit_cast(bf16x8, p1), o);
    }
    return o;
}

enum { M_NONE, M_ZERO, M_DIAG, M_TAIL };
struct AttnState { f32x16 oA, oB, mD, mT; bf16x8 qA0, qB0; bf16x4 qA1, qB1; };
__device__ __forceinline__ void exp_pack(const f32x16& s, bf16x8& p0, bf16x8& p1) {
    u32x4 a, b;
#pragma unroll
    for (int i = 0; i < 4; ++i) {
        a[i] = pack2(__builtin_amdgcn_exp2f(s[2 * i]), __builtin_amdgcn_exp2f(s[2 * i + 1]));
        b[i] = pack2(__builtin_amdgcn_exp2f(s[8 + 2 * i]), __builtin_amdgcn_exp2f(s[9 + 2 * i]));
    }
    p0 = __builtin_bit_cast(bf16x8, a); p1 = __builtin_bit_cast(bf16x8, b);
}
template <int MA, int MB>
__device__ __forceinline__ void attn_step(AttnState& st, const LAS char* kp, const LAS char* k1p, const LAS char* vp) {
    const bf16x8 k0 = *(const LAS bf16x8*)(kp);
    const bf16x4 k1 = __builtin_bit_cast(bf16x4, u32x2{*(const LAS unsigned*)(k1p), 0u});
    const bf16x8 v0 = *(const LAS bf16x8*)(vp), v1 = *(const LAS bf16x8*)(vp + 32);
    f32x16 sA, sB;
    if (MA != M_NONE) { sA = mfma32(k0, st.qA0, MA == M_ZERO ? f32x16{} : (MA == M_DIAG ? st.mD : st.mT)); sA = mfma32k8(k1, st.qA1, sA); }
    if (MB != M_NONE) { sB = mfma32(k0, st.qB0, MB == M_ZERO ? f32x16{} : (MB == M_DIAG ? st.mD : st.mT)); sB = mfma32k8(k1, st.qB1, sB); }
    bf16x8 p0, p1;
    if (MA != M_NONE) { exp_pack(sA, p0, p1); st.oA = mfma32(v0, p0, st.oA); st.oA = mfma32(v1, p1, st.oA); }
    if (MB != M_NONE) { exp_pack(sB, p0, p1); st.oB = mfma32(v0, p0, st.oB); st.oB = mfma32(v1, p1, st.oB); }
}

__device__ __forceinline__ void features(const LAS char* rowp, int lane, bf16x8 wf0, bf16x4 wf1, unsigned (&pk)[9]) {
    const int h = lane >> 5;
    bf16x8 b0 = *(const LAS bf16x8*)(rowp + 16 * h);
    unsigned t = *(const LAS unsigned*)(rowp + 32); if (h) t = 0;
    bf16x4 b1 = {(short)(t & 0xffff), (short)(t >> 16), 0, 0};
    f32x16 d = {}; d = mfma32(wf0, b0, d); d = mfma32k8(wf1, b1, d);
    float mx = d[0], mn = d[0];
#pragma unroll
    for (int i = 1; i < 9; ++i) { mx = fmaxf(mx, d[i]); mn = fminf(mn, d[i]); }
    mx = swap_max(mx); mn = swap_min(mn);
    float ep[9], en[9], sp = 0.f, sn = 0.f;
#pragma unroll
    for (int i = 0; i < 9; ++i) { ep[i] = __builtin_amdgcn_exp2f(d[i] - mx); en[i] = __builtin_amdgcn_exp2f(mn - d[i]); sp += ep[i]; sn += en[i]; }
    sp = __builtin_amdgcn_rcpf(swap_sum(sp)); sn = __builtin_amdgcn_rcpf(swap_sum(sn));
    float v[18];
#pragma unroll
    for (int i = 0; i < 9; ++i) { v[i] = ep[i] * sp; v[9 + i] = en[i] * sn; }
#pragma unroll
    for (int i = 0; i < 9; ++i) pk[i] = pack2(v[2 * i], v[2 * i + 1]);
}

__global__ __launch_bounds__(640, 3) void k_phase12(Params p) {
    extern __shared__ __attribute__((aligned(16))) char smem[];
    LAS char* lds = (LAS char*)smem;
    const int tid = threadIdx.x, lane = tid & 63, w = __builtin_amdgcn_readfirstlane(tid >> 6);
    const int bid = blockIdx.x, g = bid & 7, idx = bid >> 3, br = idx & 1, rb = idx >> 1, r0 = rb * 128;
    STAMP(0);
    u64 tag = 0;
    const __amdgpu_buffer_rsrc_t rws = __builtin_amdgcn_make_buffer_rsrc((void*)p.ws, 0, 16 << 20, 0x00020000);
    if (w >= 8) {
        if (w == 8 && lane == 0) *(LAS unsigned*)(lds + L_G + 512) = 0u;
        tag = (w == 8) ? load_loop<0>(p, lds, lane, g, br, r0) : load_loop<1>(p, lds, lane, g, br, r0);
        BAR();
        if (w == 8 && lane < 49) *(LAS u32x4*)(lds + L_V + 72 * VROW + lane * 16) = u32x4{0x3F803F80u, 0x3F803F80u, 0x3F803F80u, 0x3F803F80u};
    } else {
        f32x4 acc[4][4];
#pragma unroll
        for (int t = 0; t < 4; ++t)
#pragma unroll
            for (int n = 0; n < 4; ++n) acc[t][n] = f32x4{0, 0, 0, 0};
        unsigned a0[4], a1[4];
#define RUN(V) do { image_addr<V>(w, lane, a0, a1); gemm_loop<V>(lds, w, lane, acc); STAMP(1); BAR(); write_images<V>(lds, w, lane, br, a0, a1, acc); } while (0)
        const int v = ((w >> 1) & 1) * 2 + (w >> 2);
        if (v == 0) RUN(0); else if (v == 1) RUN(1); else if (v == 2) RUN(2); else RUN(3);
#undef RUN
    }
    BAR();
    STAMP(2);
    const int head = w & 3, h = lane >> 5, r31 = lane & 31;
    LAS char* ob = lds + L_Q;
    if (br) {
        if (w >= 8) {
            const int t2 = tid - 512;
            if (rb < 15) {
                const unsigned kg = (unsigned)WS_KG + (rb * 8 + g) * KG_BYTES;
#pragma unroll
                for (int j = 0; j < 12; ++j) { const int c = t2 + 128 * j, hd = j / 3, wi = c - 384 * hd;
                    __builtin_amdgcn_raw_buffer_store_b128(*(const LAS u32x4*)(lds + L_K + hd * 18432 + 12288 + wi * 16), rws, kg + c * 16, 0, 16); }
#pragma unroll
                for (int j = 0; j < 9; ++j) { const int c = t2 + 128 * j;
                    __builtin_amdgcn_raw_buffer_store_b128(*(const LAS u32x4*)(lds + L_V + (c >> 4) * VROW + 512 + (c & 15) * 16), rws, kg + KG_K + c * 16, 0, 16); }
            }
            WAITV(0);
            unsigned prev = 0;
            if (lane == 0) prev = __hip_atomic_fetch_add((LAS unsigned*)(lds + L_G + 512), 1u, __ATOMIC_RELAXED, __HIP_MEMORY_SCOPE_WORKGROUP);
            if (lane == 0 && prev == 1) set_flag(p.ws + WS_FKV + (rb * 8 + g) * 128, tag);
            if (rb > 0) wait_flags(p.ws + WS_FKV + ((rb - 1 - (lane & 1)) * 8 + g) * 128, lane < 2 && rb - 1 - lane >= 0, tag, p.ws);
#pragma unroll
            for (int b = 0; b < 2; ++b) {
                u32x4 v[21];
                if (rb > b) {
                    const unsigned src = (unsigned)WS_KG + ((rb - 1 - b) * 8 + g) * KG_BYTES + t2 * 16;
#pragma unroll
                    for (int j = 0; j < 21; ++j) v[j] = __builtin_amdgcn_raw_buffer_load_b128(rws, src + j * 2048, 0, 16);
#pragma unroll
                    for (int j = 0; j < 12; ++j) { const int c = t2 + 128 * j, hd = j / 3, wi = c - 384 * hd;
                        *(LAS u32x4*)(lds + L_K + hd * 18432 + (1 - b) * 6144 + wi * 16) = v[j]; }
#pragma unroll
                    for (int j = 0; j < 9; ++j) { const int c = t2 + 128 * j;
                        *(LAS u32x4*)(lds + L_V + (c >> 4) * VROW + (1 - b) * 256 + (c & 15) * 16) = v[12 + j]; }
                }
                BAR();
            }
        } else {
            AttnState st;
            const int j0 = w >> 2;
            { const LAS char* qp = lds + L_Q + head * 6144 + (32 * j0 + r31) * 48;
              st.qA0 = *(const LAS bf16x8*)(qp + 16 * h); st.qB0 = *(const LAS bf16x8*)(qp + 64 * 48 + 16 * h);
              unsigned ta = *(const LAS unsigned*)(qp + 32), tb = *(const LAS unsigned*)(qp + 64 * 48 + 32);
              st.qA1 = __builtin_bit_cast(bf16x4, u32x2{h ? 0u : ta, 0u}); st.qB1 = __builtin_bit_cast(bf16x4, u32x2{h ? 0u : tb, 0u}); }
#pragma unroll
            for (int i = 0; i < 16; ++i) { const int c = (i & 3) + 8 * (i >> 2) + 4 * h; st.mD[i] = (c <= r31) ? 0.f : NEG; st.mT[i] = (c <= r31) ? NEG : 0.f; }
            st.oA = f32x16{}; st.oB = f32x16{};
            const LAS char* kp = lds + L_K + head * 18432 + r31 * 48 + 16 * h + (j0 + 10) * 1536;
            const LAS char* k1p = lds + L_K + head * 18432 + r31 * 48 + 32 + (j0 + 10) * 1536;
            const LAS char* vp = lds + L_V + (r31 < 18 ? head * 18 + r31 : 72) * VROW + 16 * h + (j0 + 10) * 64;
#define NEXT() do { kp -= 1536; k1p -= 1536; vp -= 64; } while (0)
            attn_step<M_NONE, M_DIAG>(st, kp, k1p, vp); NEXT();
            attn_step<M_NONE, M_ZERO>(st, kp, k1p, vp); NEXT();
            attn_step<M_DIAG, M_ZERO>(st, kp, k1p, vp); NEXT();
            const int kbmin = rb >= 2 ? 0 : 8 - 4 * rb;
            int kb = j0 + 7;
            if (j0) { attn_step<M_ZERO, M_ZERO>(st, kp, k1p, vp); NEXT(); kb = 7; }
            BAR();
#pragma unroll 1
            for (; kb >= 4; --kb) { if (kb < kbmin) break; attn_step<M_ZERO, M_ZERO>(st, kp, k1p, vp); NEXT(); }
            BAR();
            if (rb >= 2) {
                if (j0 == 0) { attn_step<M_ZERO, M_ZERO>(st, kp, k1p, vp); NEXT(); }
                attn_step<M_ZERO, M_TAIL>(st, kp, k1p, vp); NEXT();
                attn_step<M_ZERO, M_NONE>(st, kp, k1p, vp); NEXT();
                attn_step<M_TAIL, M_NONE>(st, kp, k1p, vp);
            }
#undef NEXT
            const LAS char* kpb = lds + L_K + head * 18432 + r31 * 48 + 16 * h;
            const LAS char* vpb = lds + L_V + (r31 < 18 ? head * 18 + r31 : 72) * VROW + 16 * h;
#pragma unroll
            for (int it = 0; it < 2; ++it) {
                const int j = j0 + 2 * it;
                f32x16 o = it ? st.oB : st.oA;
                float l = swap_lo(o[10]);
                if (__any(!(l >= 9.3e-10f && l <= 1.07e9f))) {
                    const int J = 4 * rb + j, nb = J < 8 ? J : 8;
                    o = attn_item<true>(kpb, vpb, it ? st.qB0 : st.qA0, it ? st.qB1 : st.qA1, 8 + j - nb, 8 + j, J >= 8, lane);
                    l = swap_lo(o[10]);
                }
                const float gt = *(const LAS float*)(lds + L_G + (32 * j + r31) * 4);
                write_o(ob, lane, head, 32 * j, o, (1.0f - gt) * __builtin_amdgcn_rcpf(l));
            }
        }
    } else {
        const int th = (w >> 2) & 1, fg = lane >> 4, l15 = lane & 15;
        LAS char* kvf = lds + L_K + 2 * 18432;
        LAS char* kvb = lds + L_K + 3 * 18432;
        bf16x8 wf0; bf16x4 wf1; unsigned pq[2][9];
        f32x4 kv[3][2];
        if (w < 8) {
            { const int hh = (r31 >> 2) & 1, i = (r31 & 3) + 4 * (r31 >> 3); const bool valid = i <= 8 && r31 < 24; const int f = valid ? 9 * hh + i : 0;
              const int rot = (head & 1) ? 2 : 0; const LAS unsigned short* wp = (const LAS unsigned short*)(lds + L_WPHI) + head * 324 + f;
#pragma unroll
              for (int j = 0; j < 8; ++j) { unsigned short v = wp[(8 * h + j + rot) * 18]; wf0[j] = valid ? (short)v : (short)0; }
              const int d16 = (head & 1) ? 0 : 16;
              unsigned short a = wp[d16 * 18], b = wp[(d16 + 1) * 18];
              wf1 = (valid && h == 0) ? bf16x4{(short)a, (short)b, 0, 0} : bf16x4{0, 0, 0, 0}; }
            LAS char* pt = lds + L_K + th * 18432 + head * 2560;
#pragma unroll
            for (int a = 0; a < 3; ++a) { kv[a][0] = f32x4{0, 0, 0, 0}; kv[a][1] = f32x4{0, 0, 0, 0}; }
#pragma unroll
            for (int tb = 0; tb < 2; ++tb) {
                const int tok0 = 64 * th + 32 * tb;
                unsigned pk[9];
                features(lds + L_K + head * 18432 + (256 + tok0 + r31) * 48, lane, wf0, wf1, pk);
                LAS char* wr = pt + r31 * 80 + 40 * h;
                *(LAS u32x2*)(wr) = u32x2{pk[0], pk[1]}; *(LAS u32x2*)(wr + 8) = u32x2{pk[2], pk[3]}; *(LAS u32x2*)(wr + 16) = u32x2{pk[4], pk[5]};
                *(LAS u32x2*)(wr + 24) = u32x2{pk[6], pk[7]}; *(LAS unsigned*)(wr + 32) = pk[8];
                asm volatile("s_waitcnt lgkmcnt(0)" ::: "memory");
                const LAS char* tr = pt + (16 * (fg >> 1) + 4 * (fg & 1) + (l15 >> 2)) * 80 + 8 * (lane & 3);
                const int vr0 = head * 18 + l15, vr1 = l15 < 2 ? head * 18 + 16 + l15 : 72;
                bf16x8 vb0 = *(const LAS bf16x8*)(lds + L_V + vr0 * VROW + (256 + tok0 + 8 * fg) * 2);
                bf16x8 vb1 = *(const LAS bf16x8*)(lds + L_V + vr1 * VROW + (256 + tok0 + 8 * fg) * 2);
#pragma unroll
                for (int ft = 0; ft < 3; ++ft) {
                    bf16x8 af = cat(trread(tr + ft * 32), trread(tr + ft * 32 + 8 * 80));
                    kv[ft][0] = mfma16(af, vb0, kv[ft][0]); kv[ft][1] = mfma16(af, vb1, kv[ft][1]);
                }
                asm volatile("s_waitcnt lgkmcnt(0)" ::: "memory");
            }
            if (th == 0) {
#pragma unroll
                for (int ft = 0; ft < 3; ++ft)
#pragma unroll
                    for (int dt = 0; dt < 2; ++dt) { const int drow = 16 * dt + l15, f = 16 * ft + 4 * fg;
                        if (drow < 19 && f < 40) *(LAS f32x4*)(kvf + (head * 19 + drow) * 160 + f * 4) = kv[ft][dt]; }
            }
        }
        BAR();
        if (w < 8 && th == 1) {
#pragma unroll
            for (int ft = 0; ft < 3; ++ft)
#pragma unroll
                for (int dt = 0; dt < 2; ++dt) { const int drow = 16 * dt + l15, f = 16 * ft + 4 * fg;
                    if (drow < 19 && f < 40) { LAS f32x4* a = (LAS f32x4*)(kvf + (head * 19 + drow) * 160 + f * 4); *a = *a + kv[ft][dt]; } }
        }
        BAR();
        if (w >= 8) {
            const unsigned kvp = (unsigned)WS_KVP + (g * 16 + rb) * KVP_BYTES; const int t2 = tid - 512;
#pragma unroll
            for (int j = 0; j < 6; ++j) { const int c = t2 + 128 * j;
                __builtin_amdgcn_raw_buffer_store_b128(*(const LAS u32x4*)(kvf + c * 16), rws, c < KVP_CHUNKS ? kvp + c * 16 : 0xFFFFFF00u, 0, 16); }
            WAITV(0);
            unsigned prev = 0;
            if (lane == 0) prev = __hip_atomic_fetch_add((LAS unsigned*)(lds + L_G + 512), 1u, __ATOMIC_RELAXED, __HIP_MEMORY_SCOPE_WORKGROUP);
            if (lane == 0 && prev == 1) set_flag(p.ws + WS_FKP + (rb * 8 + g) * 128, tag);
            wait_flags(p.ws + WS_FKP + ((lane & 15) * 8 + g) * 128, lane < 16, tag, p.ws);
        } else {
#pragma unroll
            for (int tb = 0; tb < 2; ++tb) features(lds + L_Q + head * 6144 + (64 * th + 32 * tb + r31) * 48, lane, wf0, wf1, pq[tb]);
        }
        BAR();
        STAMP(4);
#pragma unroll
        for (int j = 0; j < 2; ++j) { const int c = tid + 640 * j;
            if (c < KVP_CHUNKS) {
                f32x4 s = {0, 0, 0, 0};
#pragma unroll
                for (int i = 0; i < 16; ++i) { u32x4 v = __builtin_amdgcn_raw_buffer_load_b128(rws, (unsigned)WS_KVP + (g * 16 + i) * KVP_BYTES + c * 16, 0, 16);
                    s += __builtin_bit_cast(f32x4, v); }
                const int row = c / 10, f = (c - 10 * row) * 4;
                const int fo = f >= 20 ? 48 + (f - 20) * 2 : f * 2;
                *(LAS u32x2*)(kvb + row * 96 + fo) = u32x2{pack2(s[0], s[1]), pack2(s[2], s[3])};
            } }
        BAR();
        STAMP(5);
        if (w < 8) {
            const LAS char* ap = kvb + (head * 19 + (r31 < 19 ? r31 : 18)) * 96 + 48 * h;
            const bf16x8 a0 = *(const LAS bf16x8*)(ap), a1 = *(const LAS bf16x8*)(ap + 16);
            const bf16x4 a2 = __builtin_bit_cast(bf16x4, u32x2{*(const LAS unsigned*)(ap + 32), 0u});
#pragma unroll
            for (int tb = 0; tb < 2; ++tb) {
                f32x16 o = {};
                o = mfma32(a0, __builtin_bit_cast(bf16x8, u32x4{pq[tb][0], pq[tb][1], pq[tb][2], pq[tb][3]}), o);
                o = mfma32(a1, __builtin_bit_cast(bf16x8, u32x4{pq[tb][4], pq[tb][5], pq[tb][6], pq[tb][7]}), o);
                o = mfma32k8(a2, __builtin_bit_cast(bf16x4, u32x2{pq[tb][8], 0u}), o);
                const int tok0 = 64 * th + 32 * tb;
                const float den = swap_lo(o[10]) + 1e-6f;
                const float gt = *(const LAS float*)(lds + L_G + (tok0 + r31) * 4);
                write_o(ob, lane, head, tok0, o, gt * __builtin_amdgcn_rcpf(den));
            }
        }
    }
    BAR();
    STAMP(6);
    { const unsigned od = (unsigned)WS_O + ((br * 8 + g) * 2048 + r0) * 144;
#pragma unroll
      for (int j = 0; j < 2; ++j) { const int c = tid + 640 * j;
          if (c < 1152) __builtin_amdgcn_raw_buffer_store_b128(*(const LAS u32x4*)(ob + c * 16), rws, od + c * 16, 0, 16); } }
    if (bid == 0 && w == 9) {
        bool ok = true;
#pragma unroll
        for (int j = 0; j < 2; ++j) { ok &= wait_flags(p.ws + WS_FKV + (lane + 64 * j) * 128, true, tag, p.ws); ok &= wait_flags(p.ws + WS_FKP + (lane + 64 * j) * 128, true, tag, p.ws); }
        if (lane == 0) set_flag(p.ws + WS_EPOCH, tag);
    }
    STAMP(7);
}

constexpr int P3_OB = 0, P3_W0 = 46080, P3_WQ = 36864;
__global__ __launch_bounds__(512, 2) void k_phase3(Params p) {
    extern __shared__ __attribute__((aligned(16))) char smem[];
    LAS char* lds = (LAS char*)smem;
    const int tid = threadIdx.x, lane = tid & 63, w = __builtin_amdgcn_readfirstlane(tid >> 6);
    const int bid = blockIdx.x, ct = bid % 9, rt = bid / 9, t0 = rt * 80;
    const unsigned lbase = (unsigned)(size_t)lds;
    const __amdgpu_buffer_rsrc_t ro = __builtin_amdgcn_make_buffer_rsrc((void*)(p.ws + WS_O), 0, 16 * 2048 * 144, 0x00020000);
    const __amdgpu_buffer_rsrc_t rwa = __builtin_amdgcn_make_buffer_rsrc((void*)p.woA, 0, D * D * 2, 0x00020000);
    const __amdgpu_buffer_rsrc_t rwb = __builtin_amdgcn_make_buffer_rsrc((void*)p.woB, 0, D * D * 2, 0x00020000);
    const int wrow = lane >> 3, wf = ((wrow >> 1) & 1) | (((wrow >> 3) & 1) << 1);
    auto issue_w = [&](int q) {
        const unsigned lb = lbase + P3_W0 + (q & 1) * P3_WQ;
#pragma unroll
        for (int j = 0; j < 5; ++j) { const int u = w + 8 * j;
            if (u < 36) { const int row = 8 * u + wrow, f = ((row >> 1) & 1) | (((row >> 3) & 1) << 1);
                const unsigned vo = row * 1152 + ct * 128 + (((lane & 7) ^ (f << 1)) << 4);
                glds16(lb + u * 1024, vo, q < 2 ? rwa : rwb, (q & 1) * 288 * 1152); } }
    };
    (void)wf;
    u32x4 ov[6];
    auto load_o = [&](int q) {
#pragma unroll
        for (int j = 0; j < 6; ++j) { const int c = tid + 512 * j, sl = c / 720, wi = c - 720 * sl;
            ov[j] = __builtin_amdgcn_raw_buffer_load_b128(ro, c < 2880 ? ((4 * q + sl) * 2048 + t0) * 144 + wi * 16 : 0xFFFFFF00u, 0, 16); }
    };
    load_o(0); issue_w(0);
    const int fg = lane >> 4, l15 = lane & 15;
    const int kh = w >> 2, cp = (w >> 1) & 1, tg = (w & 1) ^ kh;
    const int tt0 = tg ? 3 : 0;
    f32x4 acc[3][2];
#pragma unroll
    for (int a = 0; a < 3; ++a) { acc[a][0] = f32x4{0, 0, 0, 0}; acc[a][1] = f32x4{0, 0, 0, 0}; }
    const int fl = ((l15 >> 3) & 1) | ((fg & 1) << 1);
    const unsigned wl0 = (8 * fg + (l15 >> 2)) * 128 + (((2 * cp) ^ fl) << 5) + 8 * (lane & 3), wl1 = (8 * fg + (l15 >> 2)) * 128 + (((2 * cp + 1) ^ fl) << 5) + 8 * (lane & 3);
    for (int q = 0; q < 4; ++q) {
        BAR();
#pragma unroll
        for (int j = 0; j < 6; ++j) { const int c = tid + 512 * j; if (c < 2880) *(LAS u32x4*)(lds + P3_OB + c * 16) = ov[j]; }
        WAITV(0);
        BAR();
        if (q < 3) { load_o(q + 1); issue_w(q + 1); }
        const LAS char* wq = lds + P3_W0 + (q & 1) * P3_WQ;
        for (int ks = (kh ^ (q & 1)); ks < 9; ks += 2) {
            const int ci = 4 * ks + fg, sl = ci / 9, jj = ci - 9 * sl;
            const LAS char* op = lds + P3_OB + sl * 11520 + (16 * tt0 + l15) * 144 + jj * 16;
            bf16x8 w0 = cat(trread(wq + wl0 + ks * 4096), trread(wq + wl0 + ks * 4096 + 512));
            bf16x8 w1 = cat(trread(wq + wl1 + ks * 4096), trread(wq + wl1 + ks * 4096 + 512));
#pragma unroll
            for (int a = 0; a < 3; ++a) if (a < 2 || tg == 0) {
                bf16x8 of = *(const LAS bf16x8*)(op + a * 16 * 144);
                acc[a][0] = mfma16(w0, of, acc[a][0]); acc[a][1] = mfma16(w1, of, acc[a][1]);
            }
        }
    }
    BAR();
    LAS char* red = lds; LAS char* ot = lds + 24576;
    if (kh == 1) {
#pragma unroll
        for (int a = 0; a < 3; ++a) if (a < 2 || tg == 0)
#pragma unroll
            for (int c = 0; c < 2; ++c) *(LAS f32x4*)(red + (16 * (tt0 + a) + l15) * 272 + (32 * cp + 16 * c + 4 * fg) * 4) = acc[a][c];
    }
    BAR();
    if (kh == 0) {
#pragma unroll
        for (int a = 0; a < 3; ++a) if (a < 2 || tg == 0)
#pragma unroll
            for (int c = 0; c < 2; ++c) { f32x4 v = acc[a][c] + *(const LAS f32x4*)(red + (16 * (tt0 + a) + l15) * 272 + (32 * cp + 16 * c + 4 * fg) * 4);
                *(LAS u32x2*)(ot + (16 * (tt0 + a) + l15) * 128 + (32 * cp + 16 * c + 4 * fg) * 2) = u32x2{pack2(v[0], v[1]), pack2(v[2], v[3])}; }
    }
    BAR();
#pragma unroll
    for (int j = 0; j < 2; ++j) { const int c = tid + 512 * j, row = c >> 3;
        if (c < 640 && t0 + row < S) *(u32x4*)((char*)p.out + (size_t)(t0 + row) * 1152 + ct * 128 + (c & 7) * 16) = *(const LAS u32x4*)(ot + c * 16); }
}

extern "C" void kernel_launch(void* const* d_in, const int* in_sizes, int n_in, void* d_out, int out_size, void* d_ws, size_t ws_size, hipStream_t stream) {
    Params p;
    p.x = d_in[0]; p.wqkvA = d_in[1]; p.wphi = d_in[2]; p.woA = d_in[3]; p.wqkvB = d_in[5]; p.woB = d_in[6]; p.gw = d_in[7];
    p.out = d_out; p.ws = (char*)d_ws;
    hipFuncSetAttribute((const void*)k_phase12, hipFuncAttributeMaxDynamicSharedMemorySize, LDS_BYTES);
    hipFuncSetAttribute((const void*)k_phase3, hipFuncAttributeMaxDynamicSharedMemorySize, 120 * 1024);
    k_phase12<<<256, 640, LDS_BYTES, stream>>>(p);
    k_phase3<<<234, 512, 120 * 1024, stream>>>(p);
}
```

```cpp
#include <hip/hip_runtime.h>
#include <stdint.h>

#define LAS __attribute__((address_space(3)))
#define GAS __attribute__((address_space(1)))
typedef short bf16x8 __attribute__((ext_vector_type(8)));
typedef short bf16x4 __attribute__((ext_vector_type(4)));
typedef float f32x2 __attribute__((ext_vector_type(2)));
typedef float f32x4 __attribute__((ext_vector_type(4)));
typedef float f32x16 __attribute__((ext_vector_type(16)));
typedef unsigned u32x2 __attribute__((ext_vector_type(2)));
typedef unsigned u32x4 __attribute__((ext_vector_type(4)));
typedef __bf16 bf2_t __attribute__((ext_vector_type(2)));
typedef unsigned long long u64;

constexpr int S = 2048, D = 576;
constexpr float LOG2E = 1.4426950408889634f;
constexpr float QSCALE_B = 0.23570226039551584f * LOG2E;
constexpr float NEG = -1.0e30f;

constexpr size_t WS_EPOCH = 0, WS_FKV = 4096, WS_FKP = 20480, WS_FO = 36864, WS_TMO = 69632;
constexpr size_t WS_KG = 131072;
constexpr int KG_BYTES = 43008, KG_K = 24576;
constexpr size_t WS_KVP = 6291456;
constexpr int KVP_BYTES = 12288, KVP_CHUNKS = 760;
constexpr size_t WS_O = 8388608;
constexpr size_t WS_STAMP = 16777216;

constexpr int XS = 16384, WSB = 27648, BUFS = XS + WSB;
constexpr int L_K = 0;
constexpr int L_V = 73728, VROW = 784;
constexpr int L_Q = 130960;
constexpr int L_G = 155648;
constexpr int L_GW = 158720;
constexpr int L_WPHI = 160768;
constexpr int LDS_BYTES = 163840;
constexpr unsigned SPIN_CAP = 1u << 18;

struct Params {
    const void* x; const void* wqkvA; const void* wphi; const void* woA; const void* wqkvB; const void* woB; const void* gw;
    void* out; char* ws;
};

__device__ __forceinline__ unsigned pack2(float a, float b) { f32x2 v = {a, b}; bf2_t r = __builtin_convertvector(v, bf2_t); return __builtin_bit_cast(unsigned, r); }
__device__ __forceinline__ void glds16(unsigned ldsaddr, unsigned voff, __amdgpu_buffer_rsrc_t rsrc, unsigned soff) {
    asm volatile("s_mov_b32 m0, %0\n\ts_nop 0\n\tbuffer_load_dwordx4 %1, %2, %3 offen lds" :: "s"(ldsaddr), "v"(voff), "s"(rsrc), "s"(soff) : "memory");
}
#define BAR() asm volatile("s_waitcnt lgkmcnt(0)\n\ts_barrier" ::: "memory")
#define WAITV(n) asm volatile("s_waitcnt vmcnt(" #n ")" ::: "memory")
__device__ __forceinline__ bf16x4 trread(const LAS char* p) { typedef short v4s __attribute__((ext_vector_type(4))); return __builtin_amdgcn_ds_read_tr16_b64_v4i16((LAS v4s*)p); }
__device__ __forceinline__ bf16x8 cat(bf16x4 a, bf16x4 b) { return __builtin_shufflevector(a, b, 0, 1, 2, 3, 4, 5, 6, 7); }
__device__ __forceinline__ f32x4 mfma16(bf16x8 a, bf16x8 b, f32x4 c) { return __builtin_amdgcn_mfma_f32_16x16x32_bf16(a, b, c, 0, 0, 0); }
__device__ __forceinline__ f32x16 mfma32(bf16x8 a, bf16x8 b, f32x16 c) { return __builtin_amdgcn_mfma_f32_32x32x16_bf16(a, b, c, 0, 0, 0); }
__device__ __forceinline__ f32x16 mfma32k8(bf16x4 a, bf16x4 b, f32x16 c) { return __builtin_amdgcn_mfma_f32_32x32x8bf16_1k(a, b, c, 0, 0, 0); }
__device__ __forceinline__ float swap_lo(float v) {
    auto r = __builtin_amdgcn_permlane32_swap(__float_as_uint(v), __float_as_uint(v), false, false); return __uint_as_float(r[0]); }
__device__ __forceinline__ float swap_max(float v) { auto r = __builtin_amdgcn_permlane32_swap(__float_as_uint(v), __float_as_uint(v), false, false); return fmaxf(__uint_as_float(r[0]), __uint_as_float(r[1])); }
__device__ __forceinline__ float swap_min(float v) { auto r = __builtin_amdgcn_permlane32_swap(__float_as_uint(v), __float_as_uint(v), false, false); return fminf(__uint_as_float(r[0]), __uint_as_float(r[1])); }
__device__ __forceinline__ float swap_sum(float v) { auto r = __builtin_amdgcn_permlane32_swap(__float_as_uint(v), __float_as_uint(v), false, false); return __uint_as_float(r[0]) + __uint_as_float(r[1]); }

__device__ __forceinline__ bool wait_flags(const char* p, bool active, u64 tag, char* ws) {
    for (unsigned spins = 0;;) {
        u64 v = active ? __hip_atomic_load((const GAS u64*)p, __ATOMIC_RELAXED, __HIP_MEMORY_SCOPE_AGENT) : tag;
        if (__all(v == tag)) return true;
        __builtin_amdgcn_s_sleep(1);
        if (++spins > SPIN_CAP) { __hip_atomic_store((GAS unsigned*)(ws + WS_TMO), 1u, __ATOMIC_RELAXED, __HIP_MEMORY_SCOPE_AGENT); return false; }
    }
}
__device__ __forceinline__ void set_flag(char* p, u64 tag) { __hip_atomic_store((GAS u64*)p, tag, __ATOMIC_RELAXED, __HIP_MEMORY_SCOPE_AGENT); }

#define STAMP(i) do {} while (0)
#define STAMPW(i, ws_) do {} while (0)

template <int V> struct WT { static constexpr int T0 = V == 0 ? 0 : V == 1 ? 4 : V == 2 ? 7 : 11, NC = (V & 1) ? 3 : 4; };
constexpr int WSEC = 64 * 144;
template <int V> struct Frags { bf16x8 x[4], w[WT<V>::NC]; };
template <int V>
__device__ __forceinline__ void read_frags(Frags<V>& f, const LAS char* xp, const LAS char* wp, const LAS char* wp4, const LAS char* gp, bool gsel) {
#pragma unroll
    for (int t = 0; t < 4; ++t) f.x[t] = *(const LAS bf16x8*)(xp + t * 2048);
#pragma unroll
    for (int n = 0; n < WT<V>::NC; ++n) {
        constexpr int T0 = WT<V>::T0; const int T = T0 + n, sec = (16 * T) / 72, off = sec * WSEC + (16 * T - 72 * sec) * 2;
        if (T == 4) f.w[n] = cat(trread(wp4), trread(wp4 + 144));
        else f.w[n] = cat(trread(wp + off), trread(wp + off + 144));
    }
    if (V == 3) { const bf16x8 gf = *(const LAS bf16x8*)(gp); const u32x4 a = __builtin_bit_cast(u32x4, f.w[2]), b = __builtin_bit_cast(u32x4, gf);
        f.w[2] = __builtin_bit_cast(bf16x8, u32x4{gsel ? b[0] : a[0], gsel ? b[1] : a[1], gsel ? b[2] : a[2], gsel ? b[3] : a[3]}); }
}
template <int V>
__device__ __forceinline__ void mma_frags(const Frags<V>& f, f32x4 (&acc)[4][4]) {
#pragma unroll
    for (int n = 0; n < WT<V>::NC; ++n)
#pragma unroll
        for (int t = 0; t < 4; ++t) acc[t][n] = (WT<V>::T0 + n <= 8) ? mfma16(f.w[n], f.x[t], acc[t][n]) : mfma16(f.x[t], f.w[n], acc[t][n]);
}
template <int V>
__device__ __forceinline__ void interleave() {
#define SG1 __builtin_amdgcn_sched_group_barrier(0x008, 1, 0); __builtin_amdgcn_sched_group_barrier(0x100, 1, 0);
    SG1 SG1 SG1 SG1 SG1 SG1 SG1 SG1 SG1 SG1
    if (V != 1) { SG1 }
    if (V == 0 || V == 2) { SG1 }
#undef SG1
}
template <int V>
__device__ __forceinline__ void gemm_loop(LAS char* lds, int w, int lane, int s0, f32x4 (&acc)[4][4]) {
    const int fg = lane >> 4, l15 = lane & 15, pp = lane & 3;
    const unsigned sw = (l15 >> 1) & 7;
    const unsigned xb0 = (64 * (w & 1) + l15) * 128 + ((fg ^ sw) << 4), xb1 = xb0 ^ 64;
    const unsigned wb = XS + (8 * fg + 2 * (l15 >> 2)) * 144 + 8 * pp;
    const unsigned wb4 = wb + (pp < 2 ? 128 : WSEC - 16);
    const LAS char* gp0 = lds + L_GW + 16 * fg;
    const LAS char* gp = gp0 + s0 * 128;
    int ks = s0;
    const bool gsel = l15 >= 8;
    unsigned cur = 0;
    Frags<V> f0, f1;
    BAR();
    read_frags<V>(f0, lds + xb0, lds + wb, lds + wb4, gp, gsel);
#pragma unroll 1
    for (int s = 0; s < 9; ++s) {
        read_frags<V>(f1, lds + cur + xb1, lds + cur + wb + 32 * 144, lds + cur + wb4 + 32 * 144, gp + 64, gsel);
        mma_frags<V>(f0, acc);
        interleave<V>();
        __builtin_amdgcn_sched_barrier(0);
        cur = (cur == 2 * BUFS) ? 0 : cur + BUFS; ks = (ks == 8) ? 0 : ks + 1; gp = gp0 + ks * 128;
        if (s < 8) BAR();
        read_frags<V>(f0, lds + cur + xb0, lds + cur + wb, lds + cur + wb4, gp, gsel);
        mma_frags<V>(f1, acc);
        interleave<V>();
        __builtin_amdgcn_sched_barrier(0);
    }
}

template <int LW>
__device__ __forceinline__ u64 load_loop(const Params& p, LAS char* lds, int lane, int g, int br, int r0, int s0) {
    constexpr int NV = LW ? 4 : 5;
    const __amdgpu_buffer_rsrc_t rx = __builtin_amdgcn_make_buffer_rsrc((void*)p.x, 0, S * D * 2, 0x00020000);
    const __amdgpu_buffer_rsrc_t rw = __builtin_amdgcn_make_buffer_rsrc((void*)(br ? p.wqkvB : p.wqkvA), 0, D * 3 * D * 2, 0x00020000);
    const unsigned lbase = (unsigned)(size_t)lds;
    const unsigned vx = (r0 + 8 * LW + (lane >> 3)) * 1152 + (((lane & 7) ^ ((4 * LW + (lane >> 4)) & 7)) << 4);
    auto issue_x = [&](int s, unsigned buf) {
#pragma unroll
        for (int i = 0; i < 8; ++i) glds16(lbase + buf + LW * 1024 + i * 2048, vx, rx, s * 128 + i * (16 * 1152));
    };
    const int s1 = s0 == 8 ? 0 : s0 + 1;
    issue_x(s0, 0);
    unsigned vw[NV];
#pragma unroll
    for (int j = 0; j < NV; ++j) {
        const int ci = 64 * (LW + 2 * j) + lane, rho = (ci * 7282) >> 16, jj = ci - 9 * rho;
        const int k = (rho & ~7) + ((rho & 1) << 2) + ((rho >> 1) & 3);
        vw[j] = k * 3456 + g * 144 + jj * 16;
    }
    auto issue_w = [&](int s, unsigned buf) {
#pragma unroll
        for (int sec = 0; sec < 3; ++sec)
#pragma unroll
            for (int j = 0; j < NV; ++j) glds16(lbase + buf + XS + sec * WSEC + (LW + 2 * j) * 1024, vw[j], rw, s * (64 * 3456) + sec * 1152);
    };
    issue_w(s0, 0);
    if (LW == 0) { const __amdgpu_buffer_rsrc_t rg = __builtin_amdgcn_make_buffer_rsrc((void*)p.gw, 0, D * 2, 0x00020000);
        glds16(lbase + L_GW, lane * 16, rg, 0); glds16(lbase + L_GW + 1024, lane * 16, rg, 1024); }
    else if (!br) { const __amdgpu_buffer_rsrc_t rp = __builtin_amdgcn_make_buffer_rsrc((void*)p.wphi, 0, 32 * 324 * 2, 0x00020000);
#pragma unroll
        for (int i = 0; i < 3; ++i) glds16(lbase + L_WPHI + i * 1024, lane * 16, rp, g * 2592 + i * 1024); }
    issue_x(s1, BUFS); issue_w(s1, BUFS);
    const u64 tag = *(const __attribute__((address_space(4))) u64*)(p.ws + WS_EPOCH) + 1;
    asm volatile("" :: "s"(tag));
    unsigned nxt2 = 2 * BUFS; int k2 = s1 == 8 ? 0 : s1 + 1;
#pragma unroll 1
    for (int s = 0; s < 9; ++s) {
        if (s < 8) { if (LW) WAITV(20); else WAITV(23); } else WAITV(0);
        BAR();
        if (s + 2 < 9) { issue_x(k2, nxt2); issue_w(k2, nxt2); }
        nxt2 = (nxt2 == 2 * BUFS) ? 0 : nxt2 + BUFS; k2 = k2 == 8 ? 0 : k2 + 1;
    }
    return tag;
}

template <int V>
__device__ __forceinline__ void image_addr(int w, int lane, unsigned (&a0)[4], unsigned (&a1)[4]) {
    const int fg = lane >> 4, l15 = lane & 15;
#pragma unroll
    for (int n = 0; n < 4; ++n) { a0[n] = 0; a1[n] = 0; }
#pragma unroll
    for (int n = 0; n < WT<V>::NC; ++n) {
        const int T = WT<V>::T0 + n;
        if (T <= 8) {
            const int G = 4 * T + fg, q4 = (G * 57) >> 9, m = G - 9 * q4, sec = q4 >> 1, hp = q4 & 1;
            const int hstride = sec ? 384 * 48 : 128 * 48;
            const int base = (sec ? L_K + 256 * 48 : L_Q) + 2 * hp * hstride + (64 * (w & 1) + l15) * 48;
            a0[n] = base + (m < 4 ? 8 * m : (m == 4 ? 32 : hstride + 8 * (m - 5)));
            a1[n] = (m == 4) ? base + hstride + 32 : a0[n] + 4;
            asm volatile("" : "+v"(a0[n])); if (T & 1) asm volatile("" : "+v"(a1[n]));
        }
    }
    if (V >= 2) { a0[3] = L_V + l15 * VROW + (256 + 64 * (w & 1) + 4 * (((fg & 1) << 1) | (fg >> 1))) * 2; asm volatile("" : "+v"(a0[3])); }
}
template <int V>
__device__ __forceinline__ void write_images(LAS char* lds, int w, int lane, int br, const unsigned (&a0)[4], const unsigned (&a1)[4], const f32x4 (&acc)[4][4]) {
    const int fg = lane >> 4, l15 = lane & 15;
    const float qs = br ? QSCALE_B : LOG2E, ks = br ? 1.0f : LOG2E;
#pragma unroll
    for (int n = 0; n < WT<V>::NC; ++n) {
        const int T = WT<V>::T0 + n;
        if (T <= 8) {
            const float sc = T < 4 ? qs : (T > 4 ? ks : (fg < 2 ? qs : ks));
#pragma unroll
            for (int tt = 0; tt < 4; ++tt) {
                const unsigned lo = pack2(acc[tt][n][0] * sc, acc[tt][n][1] * sc), hi = pack2(acc[tt][n][2] * sc, acc[tt][n][3] * sc);
                if (T & 1) { *(LAS unsigned*)(lds + a0[n] + tt * 768) = lo; *(LAS unsigned*)(lds + a1[n] + tt * 768) = hi; }
                else *(LAS u32x2*)(lds + a0[n] + tt * 768) = u32x2{lo, hi};
            }
        } else if (T < 13 || l15 < 8) {
#pragma unroll
            for (int tt = 0; tt < 4; ++tt)
                *(LAS u32x2*)(lds + a0[3] + (T - 9) * 16 * VROW + tt * 32) = u32x2{pack2(acc[tt][n][0], acc[tt][n][1]), pack2(acc[tt][n][2], acc[tt][n][3])};
        }
    }
    if (V == 3 && l15 == 8) {
#pragma unroll
        for (int tt = 0; tt < 4; ++tt) *(LAS f32x4*)(lds + L_G + (64 * (w & 1) + 16 * tt + 4 * fg) * 4) = acc[tt][2];
    }
}
__device__ __forceinline__ float gate_at(const LAS char* lds, int tok) {
    return __builtin_amdgcn_rcpf(1.0f + __builtin_amdgcn_exp2f(-LOG2E * *(const LAS float*)(lds + L_G + tok * 4)));
}

__device__ __forceinline__ void write_o(LAS char* ob, int lane, int head, int tok0, const f32x16& o, float scale) {
    const int h = lane >> 5, tok = tok0 + (lane & 31);
    LAS char* a = ob + tok * 144 + head * 36 + 8 * h;
    *(LAS unsigned*)(a) = pack2(o[0] * scale, o[1] * scale);
    *(LAS unsigned*)(a + 4) = pack2(o[2] * scale, o[3] * scale);
    *(LAS unsigned*)(a + 16) = pack2(o[4] * scale, o[5] * scale);
    *(LAS unsigned*)(a + 20) = pack2(o[6] * scale, o[7] * scale);
    if (h == 0) *(LAS unsigned*)(a + 32) = pack2(o[8] * scale, o[9] * scale);
}

template <bool USEMAX>
__device__ __forceinline__ f32x16 attn_item(const LAS char* kp, const LAS char* vp, bf16x8 q0, bf16x4 q1, int kb0, int kbd, bool tail, int lane) {
    const int r = lane & 31, h = lane >> 5;
    float m = 0.f;
    if (USEMAX) {
        m = NEG;
        for (int kb = kb0; kb <= kbd; ++kb) {
            bf16x8 k0 = *(const LAS bf16x8*)(kp + kb * 1536);
            bf16x4 k1 = {0, 0, 0, 0}; { unsigned t = *(const LAS unsigned*)(kp + kb * 1536 + 32 - 16 * h); k1[0] = (short)(t & 0xffff); k1[1] = (short)(t >> 16); }
            f32x16 s = {}; s = mfma32(k0, q0, s); s = mfma32k8(k1, q1, s);
#pragma unroll
            for (int i = 0; i < 16; ++i) { const int c = (i & 3) + 8 * (i >> 2) + 4 * h;
                const bool ok = (kb == kbd) ? (c <= r) : ((tail && kb == kb0) ? (c > r) : true); m = fmaxf(m, ok ? s[i] : NEG); }
        }
        m = swap_max(m);
    }
    f32x16 o = {};
    for (int kb = kb0; kb <= kbd; ++kb) {
        bf16x8 k0 = *(const LAS bf16x8*)(kp + kb * 1536);
        bf16x4 k1 = {0, 0, 0, 0}; { unsigned t = *(const LAS unsigned*)(kp + kb * 1536 + 32 - 16 * h); k1[0] = (short)(t & 0xffff); k1[1] = (short)(t >> 16); }
        f32x16 s;
#pragma unroll
        for (int i = 0; i < 16; ++i) s[i] = -m;
        if (kb == kbd) {
#pragma unroll
            for (int i = 0; i < 16; ++i) { const int c = (i & 3) + 8 * (i >> 2) + 4 * h; s[i] = (c <= r) ? -m : NEG; }
        } else if (tail && kb == kb0) {
#pragma unroll
            for (int i = 0; i < 16; ++i) { const int c = (i & 3) + 8 * (i >> 2) + 4 * h; s[i] = (c > r) ? -m : NEG; }
        }
        s = mfma32(k0, q0, s); s = mfma32k8(k1, q1, s);
        u32x4 p0, p1;
#pragma unroll
        for (int i = 0; i < 4; ++i) {
            p0[i] = pack2(__builtin_amdgcn_exp2f(s[2 * i]), __builtin_amdgcn_exp2f(s[2 * i + 1]));
            p1[i] = pack2(__builtin_amdgcn_exp2f(s[8 + 2 * i]), __builtin_amdgcn_exp2f(s[9 + 2 * i]));
        }
        bf16x8 v0 = *(const LAS bf16x8*)(vp + kb * 64), v1 = *(const LAS bf16x8*)(vp + kb * 64 + 32);
        o = mfma32(v0, __builtin_bit_cast(bf16x8, p0), o);
        o = mfma32(v1, __builtin_bit_cast(bf16x8, p1), o);
    }
    return o;
}

enum { M_NONE, M_ZERO, M_DIAG, M_TAIL };
struct AttnState { f32x16 oA, oB, mD, mT; bf16x8 qA0, qB0; bf16x4 qA1, qB1; };
__device__ __forceinline__ void exp_pack(const f32x16& s, bf16x8& p0, bf16x8& p1) {
    u32x4 a, b;
#pragma unroll
    for (int i = 0; i < 4; ++i) {
        a[i] = pack2(__builtin_amdgcn_exp2f(s[2 * i]), __builtin_amdgcn_exp2f(s[2 * i + 1]));
        b[i] = pack2(__builtin_amdgcn_exp2f(s[8 + 2 * i]), __builtin_amdgcn_exp2f(s[9 + 2 * i]));
    }
    p0 = __builtin_bit_cast(bf16x8, a); p1 = __builtin_bit_cast(bf16x8, b);
}
template <int MA, int MB>
__device__ __forceinline__ void attn_step(AttnState& st, const LAS char* kp, const LAS char* k1p, const LAS char* vp) {
    const bf16x8 k0 = *(const LAS bf16x8*)(kp);
    const bf16x4 k1 = __builtin_bit_cast(bf16x4, u32x2{*(const LAS unsigned*)(k1p), 0u});
    const bf16x8 v0 = *(const LAS bf16x8*)(vp), v1 = *(const LAS bf16x8*)(vp + 32);
    f32x16 sA, sB;
    if (MA != M_NONE) { sA = mfma32(k0, st.qA0, MA == M_ZERO ? f32x16{} : (MA == M_DIAG ? st.mD : st.mT)); sA = mfma32k8(k1, st.qA1, sA); }
    if (MB != M_NONE) { sB = mfma32(k0, st.qB0, MB == M_ZERO ? f32x16{} : (MB == M_DIAG ? st.mD : st.mT)); sB = mfma32k8(k1, st.qB1, sB); }
    bf16x8 p0, p1;
    if (MA != M_NONE) { exp_pack(sA, p0, p1); st.oA = mfma32(v0, p0, st.oA); st.oA = mfma32(v1, p1, st.oA); }
    if (MB != M_NONE) { exp_pack(sB, p0, p1); st.oB = mfma32(v0, p0, st.oB); st.oB = mfma32(v1, p1, st.oB); }
}

__device__ __forceinline__ void features(const LAS char* rowp, int lane, bf16x8 wf0, bf16x4 wf1, unsigned (&pk)[9]) {
    const int h = lane >> 5;
    bf16x8 b0 = *(const LAS bf16x8*)(rowp + 16 * h);
    unsigned t = *(const LAS unsigned*)(rowp + 32); if (h) t = 0;
    bf16x4 b1 = {(short)(t & 0xffff), (short)(t >> 16), 0, 0};
    f32x16 d = {}; d = mfma32(wf0, b0, d); d = mfma32k8(wf1, b1, d);
    float mx = d[0], mn = d[0];
#pragma unroll
    for (int i = 1; i < 9; ++i) { mx = fmaxf(mx, d[i]); mn = fminf(mn, d[i]); }
    mx = swap_max(mx); mn = swap_min(mn);
    float ep[9], en[9], sp = 0.f, sn = 0.f;
#pragma unroll
    for (int i = 0; i < 9; ++i) { ep[i] = __builtin_amdgcn_exp2f(d[i] - mx); en[i] = __builtin_amdgcn_exp2f(mn - d[i]); sp += ep[i]; sn += en[i]; }
    sp = __builtin_amdgcn_rcpf(swap_sum(sp)); sn = __builtin_amdgcn_rcpf(swap_sum(sn));
    float v[18];
#pragma unroll
    for (int i = 0; i < 9; ++i) { v[i] = ep[i] * sp; v[9 + i] = en[i] * sn; }
#pragma unroll
    for (int i = 0; i < 9; ++i) pk[i] = pack2(v[2 * i], v[2 * i + 1]);
}

constexpr int P3_OB = 0, P3_W0 = 46080, P3_WQ = 36864;
__device__ __forceinline__ void phase3(const Params& p, LAS char* lds, int tid, int lane, int w, int rt, int ct, u64 tag) {
    const int t0 = rt * 80;
    const unsigned lbase = (unsigned)(size_t)lds;
    const __amdgpu_buffer_rsrc_t ro = __builtin_amdgcn_make_buffer_rsrc((void*)(p.ws + WS_O), 0, 16 * 2048 * 144, 0x00020000);
    const __amdgpu_buffer_rsrc_t rwa = __builtin_amdgcn_make_buffer_rsrc((void*)p.woA, 0, D * D * 2, 0x00020000);
    const __amdgpu_buffer_rsrc_t rwb = __builtin_amdgcn_make_buffer_rsrc((void*)p.woB, 0, D * D * 2, 0x00020000);
    const int wrow = lane >> 3;
    const unsigned vwo = wrow * 1152 + ct * 128 + (((lane & 7) ^ ((((wrow >> 1) & 1) | ((w & 1) << 1)) << 1)) << 4);
    auto issue_w = [&](int q) {
        const unsigned lb = lbase + P3_W0 + (q & 1) * P3_WQ + w * 1024, so = ((q & 1) * 288 + 8 * w) * 1152;
#pragma unroll
        for (int j = 0; j < 4; ++j) if (j < 3 || w < 6) glds16(lb + j * 10240, vwo, q < 2 ? rwa : rwb, so + j * 80 * 1152);
    };
    issue_w(0);
    const int rb0 = t0 >> 7, rb1 = (t0 + 79 < S ? t0 + 79 : S - 1) >> 7;
    const char* fl = p.ws + WS_FO + (((lane & 8) ? rb1 : rb0) * 16 + (lane & 7)) * 128;
    if (w == 8) wait_flags(fl, lane < 16, tag, p.ws);
    BAR();
    u32x4 ov[5];
    unsigned oo[5];
#pragma unroll
    for (int j = 0; j < 5; ++j) { const int c = tid + 640 * j, sl = (c * 5826) >> 22, wi = c - 720 * sl; oo[j] = c < 2880 ? (sl * 2048 + t0) * 144 + wi * 16 : 0xC0000000u; }
    auto load_o = [&](int q) {
#pragma unroll
        for (int j = 0; j < 5; ++j) ov[j] = __builtin_amdgcn_raw_buffer_load_b128(ro, oo[j], q * (4 * 2048 * 144), 16);
    };
    load_o(0);
    const int fg = lane >> 4, l15 = lane & 15;
    const int kh = (w >> 2) & 1, cp = (w >> 1) & 1, tg = (w & 1) ^ kh;
    const int tt0 = tg ? 3 : 0;
    f32x4 acc[3][2];
#pragma unroll
    for (int a = 0; a < 3; ++a) { acc[a][0] = f32x4{0, 0, 0, 0}; acc[a][1] = f32x4{0, 0, 0, 0}; }
    const int fl2 = ((l15 >> 3) & 1) | ((fg & 1) << 1);
    const unsigned wl0 = (8 * fg + (l15 >> 2)) * 128 + (((2 * cp) ^ fl2) << 5) + 8 * (lane & 3), wl1 = wl0 ^ 32;
    unsigned of[9];
#pragma unroll
    for (int ks = 0; ks < 9; ++ks) { const int ci = 4 * ks + fg, sl = (ci * 57) >> 9; of[ks] = P3_OB + sl * 11520 + (16 * tt0 + l15) * 144 + (ci - 9 * sl) * 16; }
#pragma unroll 1
    for (int q = 0; q < 4; ++q) {
        BAR();
#pragma unroll
        for (int j = 0; j < 5; ++j) { const int c = tid + 640 * j; if (j < 4 || c < 2880) *(LAS u32x4*)(lds + P3_OB + c * 16) = ov[j]; }
        if (q == 1 && w == 8) wait_flags(fl + 8 * 128, lane < 16, tag, p.ws);
        WAITV(0);
        BAR();
        if (q < 3) { issue_w(q + 1); load_o(q + 1); }
        if (w < 8) {
            const LAS char* wq = lds + P3_W0 + (q & 1) * P3_WQ;
            const int k0 = kh ^ (q & 1);
#pragma unroll
            for (int i = 0; i < 5; ++i) {
                if (i == 4 && k0) break;
                const int ks2 = 2 * i;
                const unsigned oa = k0 ? of[ks2 + 1 < 9 ? ks2 + 1 : 8] : of[ks2];
                const LAS char* wk = wq + (ks2 + k0) * 4096;
                bf16x8 w0 = cat(trread(wk + wl0), trread(wk + wl0 + 512));
                bf16x8 w1 = cat(trread(wk + wl1), trread(wk + wl1 + 512));
#pragma unroll
                for (int a = 0; a < 3; ++a) if (a < 2 || tg == 0) {
                    bf16x8 o8 = *(const LAS bf16x8*)(lds + oa + a * 16 * 144);
                    acc[a][0] = mfma16(w0, o8, acc[a][0]); acc[a][1] = mfma16(w1, o8, acc[a][1]);
                }
            }
        }
    }
    BAR();
    LAS char* red = lds; LAS char* ot = lds + 24576;
    if (w < 8 && kh == 1) {
#pragma unroll
        for (int a = 0; a < 3; ++a) if (a < 2 || tg == 0)
#pragma unroll
            for (int c = 0; c < 2; ++c) *(LAS f32x4*)(red + (16 * (tt0 + a) + l15) * 272 + (32 * cp + 16 * c + 4 * fg) * 4) = acc[a][c];
    }
    BAR();
    if (w < 4) {
#pragma unroll
        for (int a = 0; a < 3; ++a) if (a < 2 || tg == 0)
#pragma unroll
            for (int c = 0; c < 2; ++c) { f32x4 v = acc[a][c] + *(const LAS f32x4*)(red + (16 * (tt0 + a) + l15) * 272 + (32 * cp + 16 * c + 4 * fg) * 4);
                *(LAS u32x2*)(ot + (16 * (tt0 + a) + l15) * 128 + (32 * cp + 16 * c + 4 * fg) * 2) = u32x2{pack2(v[0], v[1]), pack2(v[2], v[3])}; }
    }
    BAR();
    { const int c = tid, row = c >> 3;
      if (t0 + row < S) *(u32x4*)((char*)p.out + (size_t)(t0 + row) * 1152 + ct * 128 + (c & 7) * 16) = *(const LAS u32x4*)(ot + c * 16); }
    STAMP(15);
}

__global__ __launch_bounds__(640, 3) void k_fused(Params p) {
    extern __shared__ __attribute__((aligned(16))) char smem[];
    LAS char* lds = (LAS char*)smem;
    const int tid = threadIdx.x, lane = tid & 63, w = __builtin_amdgcn_readfirstlane(tid >> 6);
    const int bid = blockIdx.x, g = bid & 7, idx = bid >> 3, br = idx & 1, rb = idx >> 1, r0 = rb * 128;
    const int s0 = (rb * 5 + g * 2 + br * 4) % 9;
    STAMP(0);
    u64 tag;
    const __amdgpu_buffer_rsrc_t rws = __builtin_amdgcn_make_buffer_rsrc((void*)p.ws, 0, 16 << 20, 0x00020000);
    if (w >= 8) {
        if (w == 8 && lane == 0) *(LAS unsigned*)(lds + L_G + 512) = 0u;
        tag = (w == 8) ? load_loop<0>(p, lds, lane, g, br, r0, s0) : load_loop<1>(p, lds, lane, g, br, r0, s0);
        BAR();
        if (w == 8 && lane < 49) *(LAS u32x4*)(lds + L_V + 72 * VROW + lane * 16) = u32x4{0x3F803F80u, 0x3F803F80u, 0x3F803F80u, 0x3F803F80u};
    } else {
        tag = *(const __attribute__((address_space(4))) u64*)(p.ws + WS_EPOCH) + 1;
        asm volatile("" :: "s"(tag));
        f32x4 acc[4][4];
#pragma unroll
        for (int t = 0; t < 4; ++t)
#pragma unroll
            for (int n = 0; n < 4; ++n) acc[t][n] = f32x4{0, 0, 0, 0};
        unsigned a0[4], a1[4];
#define RUN(V) do { image_addr<V>(w, lane, a0, a1); gemm_loop<V>(lds, w, lane, s0, acc); STAMP(1); BAR(); write_images<V>(lds, w, lane, br, a0, a1, acc); } while (0)
        const int v = ((w >> 1) & 1) * 2 + (w >> 2);
        if (v == 0) RUN(0); else if (v == 1) RUN(1); else if (v == 2) RUN(2); else RUN(3);
#undef RUN
    }
    BAR();
    STAMP(2);
    const int head = w & 3, h = lane >> 5, r31 = lane & 31;
    LAS char* ob = lds + L_Q;
    if (br) {
        if (rb < 15) {
            const unsigned kg = (unsigned)WS_KG + (rb * 8 + g) * KG_BYTES;
#pragma unroll
            for (int j = 0; j < 5; ++j) { const int c = tid + 640 * j;
                if (j < 4 || c < 2688) {
                    int a;
                    if (j < 2 || (j == 2 && c < 1536)) { const int hd = (c * 683) >> 18, wi = c - 384 * hd; a = L_K + hd * 18432 + 12288 + wi * 16; }
                    else { const int cc = c - 1536; a = L_V + (cc >> 4) * VROW + 512 + (cc & 15) * 16; }
                    __builtin_amdgcn_raw_buffer_store_b128(*(const LAS u32x4*)(lds + a), rws, kg + c * 16, 0, 16);
                } }
        }
#define KV_ARRIVE() do { WAITV(0); unsigned prev = 0; \
            if (lane == 0) prev = __hip_atomic_fetch_add((LAS unsigned*)(lds + L_G + 512), 1u, __ATOMIC_RELAXED, __HIP_MEMORY_SCOPE_WORKGROUP); \
            if (lane == 0 && prev == 9) set_flag(p.ws + WS_FKV + (rb * 8 + g) * 128, tag); } while (0)
        if (w >= 8) {
            const int t2 = tid - 512;
            KV_ARRIVE();
            if (w == 8) STAMPW(11, p.ws);
            if (rb > 0) wait_flags(p.ws + WS_FKV + ((rb - 1 - (lane & 1)) * 8 + g) * 128, lane < 2 && rb - 1 - lane >= 0, tag, p.ws);
            if (w == 8) STAMPW(12, p.ws);
#pragma unroll
            for (int b = 0; b < 2; ++b) {
                u32x4 v[21];
                if (rb > b) {
                    const unsigned src = (unsigned)WS_KG + ((rb - 1 - b) * 8 + g) * KG_BYTES + t2 * 16;
#pragma unroll
                    for (int j = 0; j < 21; ++j) v[j] = __builtin_amdgcn_raw_buffer_load_b128(rws, src + j * 2048, 0, 16);
#pragma unroll
                    for (int j = 0; j < 12; ++j) { const int c = t2 + 128 * j, hd = j / 3, wi = c - 384 * hd;
                        *(LAS u32x4*)(lds + L_K + hd * 18432 + (1 - b) * 6144 + wi * 16) = v[j]; }
#pragma unroll
                    for (int j = 0; j < 9; ++j) { const int c = t2 + 128 * j;
                        *(LAS u32x4*)(lds + L_V + (c >> 4) * VROW + (1 - b) * 256 + (c & 15) * 16) = v[12 + j]; }
                }
                if (w == 8) STAMPW(13 + b, p.ws);
                BAR();
            }
        } else {
            AttnState st;
            const int j0 = w >> 2;
            { const LAS char* qp = lds + L_Q + head * 6144 + (32 * j0 + r31) * 48;
              st.qA0 = *(const LAS bf16x8*)(qp + 16 * h); st.qB0 = *(const LAS bf16x8*)(qp + 64 * 48 + 16 * h);
              unsigned ta = *(const LAS unsigned*)(qp + 32), tb = *(const LAS unsigned*)(qp + 64 * 48 + 32);
              st.qA1 = __builtin_bit_cast(bf16x4, u32x2{h ? 0u : ta, 0u}); st.qB1 = __builtin_bit_cast(bf16x4, u32x2{h ? 0u : tb, 0u}); }
#pragma unroll
            for (int i = 0; i < 16; ++i) { const int c = (i & 3) + 8 * (i >> 2) + 4 * h; st.mD[i] = (c <= r31) ? 0.f : NEG; st.mT[i] = (c <= r31) ? NEG : 0.f; }
            st.oA = f32x16{}; st.oB = f32x16{};
            const LAS char* kp = lds + L_K + head * 18432 + r31 * 48 + 16 * h + (j0 + 10) * 1536;
            const LAS char* k1p = lds + L_K + head * 18432 + r31 * 48 + 32 + (j0 + 10) * 1536;
            const LAS char* vp = lds + L_V + (r31 < 18 ? head * 18 + r31 : 72) * VROW + 16 * h + (j0 + 10) * 64;
#define NEXT() do { kp -= 1536; k1p -= 1536; vp -= 64; } while (0)
            attn_step<M_NONE, M_DIAG>(st, kp, k1p, vp); NEXT();
            KV_ARRIVE();
            attn_step<M_NONE, M_ZERO>(st, kp, k1p, vp); NEXT();
            attn_step<M_DIAG, M_ZERO>(st, kp, k1p, vp); NEXT();
            const int kbmin = rb >= 2 ? 0 : 8 - 4 * rb;
            int kb = j0 + 7;
            if (j0) { attn_step<M_ZERO, M_ZERO>(st, kp, k1p, vp); NEXT(); kb = 7; }
            STAMP(3);
            BAR();
            STAMP(4);
#pragma unroll 1
            for (; kb >= 4; --kb) { if (kb < kbmin) break; attn_step<M_ZERO, M_ZERO>(st, kp, k1p, vp); NEXT(); }
            BAR();
            STAMP(5);
            if (rb >= 2) {
                if (j0 == 0) { attn_step<M_ZERO, M_ZERO>(st, kp, k1p, vp); NEXT(); }
                attn_step<M_ZERO, M_TAIL>(st, kp, k1p, vp); NEXT();
                attn_step<M_ZERO, M_NONE>(st, kp, k1p, vp); NEXT();
                attn_step<M_TAIL, M_NONE>(st, kp, k1p, vp);
            }
#undef NEXT
            const LAS char* kpb = lds + L_K + head * 18432 + r31 * 48 + 16 * h;
            const LAS char* vpb = lds + L_V + (r31 < 18 ? head * 18 + r31 : 72) * VROW + 16 * h;
#pragma unroll
            for (int it = 0; it < 2; ++it) {
                const int j = j0 + 2 * it;
                f32x16 o = it ? st.oB : st.oA;
                float l = swap_lo(o[10]);
                if (__any(!(l >= 9.3e-10f && l <= 1.07e9f))) {
                    const int J = 4 * rb + j, nb = J < 8 ? J : 8;
                    o = attn_item<true>(kpb, vpb, it ? st.qB0 : st.qA0, it ? st.qB1 : st.qA1, 8 + j - nb, 8 + j, J >= 8, lane);
                    l = swap_lo(o[10]);
                }
                const float gt = gate_at(lds, 32 * j + r31);
                write_o(ob, lane, head, 32 * j, o, (1.0f - gt) * __builtin_amdgcn_rcpf(l));
            }
        }
    } else {
        const int th = (w >> 2) & 1, fg = lane >> 4, l15 = lane & 15;
        LAS char* kvf = lds + L_K + 2 * 18432;
        LAS char* kvb = lds + L_K + 3 * 18432;
        bf16x8 wf0; bf16x4 wf1; unsigned pq[2][9];
        f32x4 kv[3][2];
        if (w < 8) {
            { const int hh = (r31 >> 2) & 1, i = (r31 & 3) + 4 * (r31 >> 3); const bool valid = i <= 8 && r31 < 24; const int f = valid ? 9 * hh + i : 0;
              const int rot = (head & 1) ? 2 : 0; const LAS unsigned short* wp = (const LAS unsigned short*)(lds + L_WPHI) + head * 324 + f;
#pragma unroll
              for (int j = 0; j < 8; ++j) { unsigned short v = wp[(8 * h + j + rot) * 18]; wf0[j] = valid ? (short)v : (short)0; }
              const int d16 = (head & 1) ? 0 : 16;
              unsigned short a = wp[d16 * 18], b = wp[(d16 + 1) * 18];
              wf1 = (valid && h == 0) ? bf16x4{(short)a, (short)b, 0, 0} : bf16x4{0, 0, 0, 0}; }
            LAS char* pt = lds + L_K + th * 18432 + head * 2560;
#pragma unroll
            for (int a = 0; a < 3; ++a) { kv[a][0] = f32x4{0, 0, 0, 0}; kv[a][1] = f32x4{0, 0, 0, 0}; }
#pragma unroll
            for (int tb = 0; tb < 2; ++tb) {
                const int tok0 = 64 * th + 32 * tb;
                unsigned pk[9];
                features(lds + L_K + head * 18432 + (256 + tok0 + r31) * 48, lane, wf0, wf1, pk);
                LAS char* wr = pt + r31 * 80 + 40 * h;
                *(LAS u32x2*)(wr) = u32x2{pk[0], pk[1]}; *(LAS u32x2*)(wr + 8) = u32x2{pk[2], pk[3]}; *(LAS u32x2*)(wr + 16) = u32x2{pk[4], pk[5]};
                *(LAS u32x2*)(wr + 24) = u32x2{pk[6], pk[7]}; *(LAS unsigned*)(wr + 32) = pk[8];
                asm volatile("s_waitcnt lgkmcnt(0)" ::: "memory");
                const LAS char* tr = pt + (16 * (fg >> 1) + 4 * (fg & 1) + (l15 >> 2)) * 80 + 8 * (lane & 3);
                const int vr0 = head * 18 + l15, vr1 = l15 < 2 ? head * 18 + 16 + l15 : 72;
                bf16x8 vb0 = *(const LAS bf16x8*)(lds + L_V + vr0 * VROW + (256 + tok0 + 8 * fg) * 2);
                bf16x8 vb1 = *(const LAS bf16x8*)(lds + L_V + vr1 * VROW + (256 + tok0 + 8 * fg) * 2);
#pragma unroll
                for (int ft = 0; ft < 3; ++ft) {
                    bf16x8 af = cat(trread(tr + ft * 32), trread(tr + ft * 32 + 8 * 80));
                    kv[ft][0] = mfma16(af, vb0, kv[ft][0]); kv[ft][1] = mfma16(af, vb1, kv[ft][1]);
                }
                asm volatile("s_waitcnt lgkmcnt(0)" ::: "memory");
            }
            if (th == 0) {
#pragma unroll
                for (int ft = 0; ft < 3; ++ft)
#pragma unroll
                    for (int dt = 0; dt < 2; ++dt) { const int drow = 16 * dt + l15, f = 16 * ft + 4 * fg;
                        if (drow < 19 && f < 40) *(LAS f32x4*)(kvf + (head * 19 + drow) * 160 + f * 4) = kv[ft][dt]; }
            }
        }
        BAR();
        if (w < 8 && th == 1) {
#pragma unroll
            for (int ft = 0; ft < 3; ++ft)
#pragma unroll
                for (int dt = 0; dt < 2; ++dt) { const int drow = 16 * dt + l15, f = 16 * ft + 4 * fg;
                    if (drow < 19 && f < 40) { LAS f32x4* a = (LAS f32x4*)(kvf + (head * 19 + drow) * 160 + f * 4); *a = *a + kv[ft][dt]; } }
        }
        BAR();
        if (w >= 8) {
            const unsigned kvp = (unsigned)WS_KVP + (g * 16 + rb) * KVP_BYTES; const int t2 = tid - 512;
#pragma unroll
            for (int j = 0; j < 6; ++j) { const int c = t2 + 128 * j;
                __builtin_amdgcn_raw_buffer_store_b128(*(const LAS u32x4*)(kvf + c * 16), rws, c < KVP_CHUNKS ? kvp + c * 16 : 0xFFFFFF00u, 0, 16); }
            WAITV(0);
            unsigned prev = 0;
            if (lane == 0) prev = __hip_atomic_fetch_add((LAS unsigned*)(lds + L_G + 512), 1u, __ATOMIC_RELAXED, __HIP_MEMORY_SCOPE_WORKGROUP);
            if (lane == 0 && prev == 1) set_flag(p.ws + WS_FKP + (rb * 8 + g) * 128, tag);
            wait_flags(p.ws + WS_FKP + ((lane & 15) * 8 + g) * 128, lane < 16, tag, p.ws);
        } else {
#pragma unroll
            for (int tb = 0; tb < 2; ++tb) features(lds + L_Q + head * 6144 + (64 * th + 32 * tb + r31) * 48, lane, wf0, wf1, pq[tb]);
        }
        BAR();
        STAMP(4);
#pragma unroll
        for (int j = 0; j < 2; ++j) { const int c = tid + 640 * j;
            if (c < KVP_CHUNKS) {
                f32x4 s = {0, 0, 0, 0};
#pragma unroll
                for (int i = 0; i < 16; ++i) { u32x4 v = __builtin_amdgcn_raw_buffer_load_b128(rws, (unsigned)WS_KVP + (g * 16 + i) * KVP_BYTES + c * 16, 0, 16);
                    s += __builtin_bit_cast(f32x4, v); }
                const int row = c / 10, f = (c - 10 * row) * 4;
                const int fo = f >= 20 ? 48 + (f - 20) * 2 : f * 2;
                *(LAS u32x2*)(kvb + row * 96 + fo) = u32x2{pack2(s[0], s[1]), pack2(s[2], s[3])};
            } }
        BAR();
        STAMP(5);
        if (w < 8) {
            const LAS char* ap = kvb + (head * 19 + (r31 < 19 ? r31 : 18)) * 96 + 48 * h;
            const bf16x8 a0 = *(const LAS bf16x8*)(ap), a1 = *(const LAS bf16x8*)(ap + 16);
            const bf16x4 a2 = __builtin_bit_cast(bf16x4, u32x2{*(const LAS unsigned*)(ap + 32), 0u});
#pragma unroll
            for (int tb = 0; tb < 2; ++tb) {
                f32x16 o = {};
                o = mfma32(a0, __builtin_bit_cast(bf16x8, u32x4{pq[tb][0], pq[tb][1], pq[tb][2], pq[tb][3]}), o);
                o = mfma32(a1, __builtin_bit_cast(bf16x8, u32x4{pq[tb][4], pq[tb][5], pq[tb][6], pq[tb][7]}), o);
                o = mfma32k8(a2, __builtin_bit_cast(bf16x4, u32x2{pq[tb][8], 0u}), o);
                const int tok0 = 64 * th + 32 * tb;
                const float den = swap_lo(o[10]) + 1e-6f;
                const float gt = gate_at(lds, tok0 + r31);
                write_o(ob, lane, head, tok0, o, gt * __builtin_amdgcn_rcpf(den));
            }
        }
    }
    BAR();
    STAMP(6);
    { const unsigned od = (unsigned)WS_O + ((br * 8 + g) * 2048 + r0) * 144;
#pragma unroll
      for (int j = 0; j < 2; ++j) { const int c = tid + 640 * j;
          if (c < 1152) __builtin_amdgcn_raw_buffer_store_b128(*(const LAS u32x4*)(ob + c * 16), rws, od + c * 16, 0, 16); } }
    WAITV(0);
    BAR();
    if (tid == 0) set_flag(p.ws + WS_FO + (rb * 16 + br * 8 + g) * 128, tag);
    STAMP(7);
    if (bid == 0 && w == 9) {
        for (unsigned spins = 0;;) {
            bool ok = true;
#pragma unroll
            for (int j = 0; j < 4; ++j) ok &= __hip_atomic_load((const GAS u64*)(p.ws + (j < 2 ? WS_FKV : WS_FKP - 16384) + (lane + 64 * j) * 128), __ATOMIC_RELAXED, __HIP_MEMORY_SCOPE_AGENT) == tag;
            if (__all(ok)) { if (lane == 0) set_flag(p.ws + WS_EPOCH, tag); break; }
            if (++spins > SPIN_CAP) break;
            __builtin_amdgcn_s_sleep(1);
        }
    }
    { const int xcd = bid & 7, ii = bid >> 3; int rt, ct;
      if (ii < 27) { rt = xcd * 3 + ii / 9; ct = ii % 9; } else { const int sp = xcd * 5 + ii - 27; rt = 24 + sp / 9; ct = sp % 9; if (sp >= 18) return; }
      phase3(p, lds, tid, lane, w, rt, ct, tag);
    }
}

extern "C" void kernel_launch(void* const* d_in, const int* in_sizes, int n_in, void* d_out, int out_size, void* d_ws, size_t ws_size, hipStream_t stream) {
    Params p;
    p.x = d_in[0]; p.wqkvA = d_in[1]; p.wphi = d_in[2]; p.woA = d_in[3]; p.wqkvB = d_in[5]; p.woB = d_in[6]; p.gw = d_in[7];
    p.out = d_out; p.ws = (char*)d_ws;
    hipFuncSetAttribute((const void*)k_fused, hipFuncAttributeMaxDynamicSharedMemorySize, LDS_BYTES);
    k_fused<<<256, 640, LDS_BYTES, stream>>>(p);
}
```

```cpp
#include <hip/hip_runtime.h>
#include <stdint.h>
#include <type_traits>
#define ROT 1

#define LAS __attribute__((address_space(3)))
#define GAS __attribute__((address_space(1)))
typedef short bf16x8 __attribute__((ext_vector_type(8)));
typedef short bf16x4 __attribute__((ext_vector_type(4)));
typedef float f32x2 __attribute__((ext_vector_type(2)));
typedef float f32x4 __attribute__((ext_vector_type(4)));
typedef float f32x16 __attribute__((ext_vector_type(16)));
typedef unsigned u32x2 __attribute__((ext_vector_type(2)));
typedef unsigned u32x4 __attribute__((ext_vector_type(4)));
typedef __bf16 bf2_t __attribute__((ext_vector_type(2)));
typedef unsigned long long u64;

constexpr int S = 2048, D = 576;
constexpr float LOG2E = 1.4426950408889634f;
constexpr float QSCALE_B = 0.23570226039551584f * LOG2E;
constexpr float NEG = -1.0e30f;

constexpr size_t WS_EPOCH = 0, WS_FKV = 4096, WS_FKP = 20480, WS_FO = 36864, WS_TMO = 69632;
constexpr size_t WS_KG = 131072;
constexpr int KG_BYTES = 43008, KG_K = 24576;
constexpr size_t WS_KVP = 6291456;
constexpr int KVP_BYTES = 6144, KVP_CHUNKS = 380;
constexpr size_t WS_O = 8388608;
constexpr size_t WS_STAMP = 16777216;

constexpr int XS = 16384, WSB = 27648, BUFS = XS + WSB;
constexpr int L_K = 0;
constexpr int L_V = 73728, VROW = 784;
constexpr int L_Q = 130960;
constexpr int L_G = 155648;
constexpr int L_GW = 158720;
constexpr int L_WPHI = 160768;
constexpr int LDS_BYTES = 163840;
constexpr unsigned SPIN_CAP = 1u << 18;

struct Params {
    const void* x; const void* wqkvA; const void* wphi; const void* woA; const void* wqkvB; const void* woB; const void* gw;
    void* out; char* ws;
};

__device__ __forceinline__ unsigned pack2(float a, float b) { f32x2 v = {a, b}; bf2_t r = __builtin_convertvector(v, bf2_t); return __builtin_bit_cast(unsigned, r); }
__device__ __forceinline__ void glds16(unsigned ldsaddr, unsigned voff, __amdgpu_buffer_rsrc_t rsrc, unsigned soff) {
    asm volatile("s_mov_b32 m0, %0\n\ts_nop 0\n\tbuffer_load_dwordx4 %1, %2, %3 offen lds" :: "s"(ldsaddr), "v"(voff), "s"(rsrc), "s"(soff) : "memory");
}
#define BAR() asm volatile("s_waitcnt lgkmcnt(0)\n\ts_barrier" ::: "memory")
#define WAITV(n) asm volatile("s_waitcnt vmcnt(" #n ")" ::: "memory")
__device__ __forceinline__ bf16x4 trread(const LAS char* p) { typedef short v4s __attribute__((ext_vector_type(4))); return __builtin_amdgcn_ds_read_tr16_b64_v4i16((LAS v4s*)p); }
__device__ __forceinline__ bf16x8 cat(bf16x4 a, bf16x4 b) { return __builtin_shufflevector(a, b, 0, 1, 2, 3, 4, 5, 6, 7); }
__device__ __forceinline__ f32x4 mfma16(bf16x8 a, bf16x8 b, f32x4 c) { return __builtin_amdgcn_mfma_f32_16x16x32_bf16(a, b, c, 0, 0, 0); }
__device__ __forceinline__ f32x16 mfma32(bf16x8 a, bf16x8 b, f32x16 c) { return __builtin_amdgcn_mfma_f32_32x32x16_bf16(a, b, c, 0, 0, 0); }
__device__ __forceinline__ f32x16 mfma32k8(bf16x4 a, bf16x4 b, f32x16 c) { return __builtin_amdgcn_mfma_f32_32x32x8bf16_1k(a, b, c, 0, 0, 0); }
__device__ __forceinline__ float swap_lo(float v) {
    auto r = __builtin_amdgcn_permlane32_swap(__float_as_uint(v), __float_as_uint(v), false, false); return __uint_as_float(r[0]); }
__device__ __forceinline__ float swap_max(float v) { auto r = __builtin_amdgcn_permlane32_swap(__float_as_uint(v), __float_as_uint(v), false, false); return fmaxf(__uint_as_float(r[0]), __uint_as_float(r[1])); }
__device__ __forceinline__ float swap_min(float v) { auto r = __builtin_amdgcn_permlane32_swap(__float_as_uint(v), __float_as_uint(v), false, false); return fminf(__uint_as_float(r[0]), __uint_as_float(r[1])); }
__device__ __forceinline__ float swap_sum(float v) { auto r = __builtin_amdgcn_permlane32_swap(__float_as_uint(v), __float_as_uint(v), false, false); return __uint_as_float(r[0]) + __uint_as_float(r[1]); }

__device__ __forceinline__ bool wait_flags(const char* p, bool active, u64 tag, char* ws) {
    for (unsigned spins = 0;;) {
        u64 v = active ? __hip_atomic_load((const GAS u64*)p, __ATOMIC_RELAXED, __HIP_MEMORY_SCOPE_AGENT) : tag;
        if (__all(v == tag)) return true;
        __builtin_amdgcn_s_sleep(1);
        if (++spins > SPIN_CAP) { __hip_atomic_store((GAS unsigned*)(ws + WS_TMO), 1u, __ATOMIC_RELAXED, __HIP_MEMORY_SCOPE_AGENT); return false; }
    }
}
__device__ __forceinline__ void set_flag(char* p, u64 tag) { __hip_atomic_store((GAS u64*)p, tag, __ATOMIC_RELAXED, __HIP_MEMORY_SCOPE_AGENT); }

#define STAMP(i) do {} while (0)
#define STAMP9(i) do {} while (0)
#define STAMPW(i, ws_) do {} while (0)
#define STAMP_DUMP() do {} while (0)
#define STAMP_INIT() do {} while (0)

template <int V> struct WT { static constexpr int T0 = V == 0 ? 0 : V == 1 ? 4 : V == 2 ? 7 : 11, NC = (V & 1) ? 3 : 4; };
constexpr int WSEC = 64 * 144;
template <int V> struct Frags { bf16x8 x[4], w[WT<V>::NC]; };
template <int V>
__device__ __forceinline__ void read_frags(Frags<V>& f, const LAS char* xp, const LAS char* wp, const LAS char* wp4, const LAS char* gp, bool gsel) {
#pragma unroll
    for (int t = 0; t < 4; ++t) f.x[t] = *(const LAS bf16x8*)(xp + t * 2048);
#pragma unroll
    for (int n = 0; n < WT<V>::NC; ++n) {
        constexpr int T0 = WT<V>::T0; const int T = T0 + n, sec = (16 * T) / 72, off = sec * WSEC + (16 * T - 72 * sec) * 2;
        if (T == 4) f.w[n] = cat(trread(wp4), trread(wp4 + 144));
        else f.w[n] = cat(trread(wp + off), trread(wp + off + 144));
    }
    if (V == 3) { const bf16x8 gf = *(const LAS bf16x8*)(gp); const u32x4 a = __builtin_bit_cast(u32x4, f.w[2]), b = __builtin_bit_cast(u32x4, gf);
        f.w[2] = __builtin_bit_cast(bf16x8, u32x4{gsel ? b[0] : a[0], gsel ? b[1] : a[1], gsel ? b[2] : a[2], gsel ? b[3] : a[3]}); }
}
template <int V>
__device__ __forceinline__ void mma_frags(const Frags<V>& f, f32x4 (&acc)[4][4]) {
#pragma unroll
    for (int n = 0; n < WT<V>::NC; ++n)
#pragma unroll
        for (int t = 0; t < 4; ++t) acc[t][n] = (WT<V>::T0 + n <= 8) ? mfma16(f.w[n], f.x[t], acc[t][n]) : mfma16(f.x[t], f.w[n], acc[t][n]);
}
template <int V>
__device__ __forceinline__ void interleave() {
#define SG1 __builtin_amdgcn_sched_group_barrier(0x008, 1, 0); __builtin_amdgcn_sched_group_barrier(0x100, 1, 0);
    SG1 SG1 SG1 SG1 SG1 SG1 SG1 SG1 SG1 SG1
    if (V != 1) { SG1 }
    if (V == 0 || V == 2) { SG1 }
#undef SG1
}
template <int V>
__device__ __forceinline__ void gemm_loop(LAS char* lds, int w, int lane, int s0, f32x4 (&acc)[4][4]) {
    const int fg = lane >> 4, l15 = lane & 15, pp = lane & 3;
    const unsigned sw = (l15 >> 1) & 7;
    const unsigned xb0 = (64 * (w & 1) + l15) * 128 + ((fg ^ sw) << 4), xb1 = xb0 ^ 64;
    const unsigned wb = XS + (8 * fg + 2 * (l15 >> 2)) * 144 + 8 * pp;
    const unsigned wb4 = wb + (pp < 2 ? 128 : WSEC - 16);
    const LAS char* gp0 = lds + L_GW + 16 * fg;
    const LAS char* gp = gp0 + s0 * 128;
    int ks = s0;
    const bool gsel = l15 >= 8;
    unsigned cur = 0;
    Frags<V> f0, f1;
    BAR();
    read_frags<V>(f0, lds + xb0, lds + wb, lds + wb4, gp, gsel);
#pragma unroll 1
    for (int s = 0; s < 9; ++s) {
        read_frags<V>(f1, lds + cur + xb1, lds + cur + wb + 32 * 144, lds + cur + wb4 + 32 * 144, gp + 64, gsel);
        mma_frags<V>(f0, acc);
        interleave<V>();
        __builtin_amdgcn_sched_barrier(0);
        cur = (cur == 2 * BUFS) ? 0 : cur + BUFS; ks = (ks == 8) ? 0 : ks + 1; gp = gp0 + ks * 128;
        if (s < 8) BAR();
        read_frags<V>(f0, lds + cur + xb0, lds + cur + wb, lds + cur + wb4, gp, gsel);
        mma_frags<V>(f1, acc);
        interleave<V>();
        __builtin_amdgcn_sched_barrier(0);
    }
}

template <int LW>
__device__ __forceinline__ u64 load_loop(const Params& p, LAS char* lds, int lane, int g, int br, int r0, int s0) {
    constexpr int NV = LW ? 4 : 5;
    const __amdgpu_buffer_rsrc_t rx = __builtin_amdgcn_make_buffer_rsrc((void*)p.x, 0, S * D * 2, 0x00020000);
    const __amdgpu_buffer_rsrc_t rw = __builtin_amdgcn_make_buffer_rsrc((void*)(br ? p.wqkvB : p.wqkvA), 0, D * 3 * D * 2, 0x00020000);
    const unsigned lbase = (unsigned)(size_t)lds;
    const unsigned vx = (r0 + 8 * LW + (lane >> 3)) * 1152 + (((lane & 7) ^ ((4 * LW + (lane >> 4)) & 7)) << 4);
    auto issue_x = [&](int s, unsigned buf) {
#pragma unroll
        for (int i = 0; i < 8; ++i) glds16(lbase + buf + LW * 1024 + i * 2048, vx, rx, s * 128 + i * (16 * 1152));
    };
    STAMP(8);
    const int s1 = s0 == 8 ? 0 : s0 + 1;
    issue_x(s0, 0);
    unsigned vw[NV];
#pragma unroll
    for (int j = 0; j < NV; ++j) {
        const int ci = 64 * (LW + 2 * j) + lane, rho = (ci * 7282) >> 16, jj = ci - 9 * rho;
        const int k = (rho & ~7) + ((rho & 1) << 2) + ((rho >> 1) & 3);
        vw[j] = k * 3456 + g * 144 + jj * 16;
    }
    auto issue_w = [&](int s, unsigned buf) {
#pragma unroll
        for (int sec = 0; sec < 3; ++sec)
#pragma unroll
            for (int j = 0; j < NV; ++j) glds16(lbase + buf + XS + sec * WSEC + (LW + 2 * j) * 1024, vw[j], rw, s * (64 * 3456) + sec * 1152);
    };
    issue_w(s0, 0);
    if (LW == 0) { const __amdgpu_buffer_rsrc_t rg = __builtin_amdgcn_make_buffer_rsrc((void*)p.gw, 0, D * 2, 0x00020000);
        glds16(lbase + L_GW, lane * 16, rg, 0); glds16(lbase + L_GW + 1024, lane * 16, rg, 1024); }
    else if (!br) { const __amdgpu_buffer_rsrc_t rp = __builtin_amdgcn_make_buffer_rsrc((void*)p.wphi, 0, 32 * 324 * 2, 0x00020000);
#pragma unroll
        for (int i = 0; i < 3; ++i) glds16(lbase + L_WPHI + i * 1024, lane * 16, rp, g * 2592 + i * 1024); }
    issue_x(s1, BUFS); issue_w(s1, BUFS);
    STAMP(9);
    const u64 tag = *(const __attribute__((address_space(4))) u64*)(p.ws + WS_EPOCH) + 1;
    asm volatile("" :: "s"(tag));
    unsigned nxt2 = 2 * BUFS; int k2 = s1 == 8 ? 0 : s1 + 1;
#pragma unroll 1
    for (int s = 0; s < 9; ++s) {
        if (s < 8) { if (LW) WAITV(20); else WAITV(23); } else WAITV(0);
        if (s == 0) STAMP(10);
        STAMP9(s);
        BAR();
        STAMP9(9 + s);
        if (s + 2 < 9) { issue_x(k2, nxt2); issue_w(k2, nxt2); }
        nxt2 = (nxt2 == 2 * BUFS) ? 0 : nxt2 + BUFS; k2 = k2 == 8 ? 0 : k2 + 1;
    }
    return tag;
}

template <int V>
__device__ __forceinline__ void image_addr(int w, int lane, unsigned (&a0)[4], unsigned (&a1)[4]) {
    const int fg = lane >> 4, l15 = lane & 15;
#pragma unroll
    for (int n = 0; n < 4; ++n) { a0[n] = 0; a1[n] = 0; }
#pragma unroll
    for (int n = 0; n < WT<V>::NC; ++n) {
        const int T = WT<V>::T0 + n;
        if (T <= 8) {
            const int G = 4 * T + fg, q4 = (G * 57) >> 9, m = G - 9 * q4, sec = q4 >> 1, hp = q4 & 1;
            const int hstride = sec ? 384 * 48 : 128 * 48;
            const int base = (sec ? L_K + 256 * 48 : L_Q) + 2 * hp * hstride + (64 * (w & 1) + l15) * 48;
            a0[n] = base + (m < 4 ? 8 * m : (m == 4 ? 32 : hstride + 8 * (m - 5)));
            a1[n] = (m == 4) ? base + hstride + 32 : a0[n] + 4;
            asm volatile("" : "+v"(a0[n])); if (T & 1) asm volatile("" : "+v"(a1[n]));
        }
    }
    if (V >= 2) { a0[3] = L_V + l15 * VROW + (256 + 64 * (w & 1) + 4 * (((fg & 1) << 1) | (fg >> 1))) * 2; asm volatile("" : "+v"(a0[3])); }
}
template <int V>
__device__ __forceinline__ void write_images(LAS char* lds, int w, int lane, int br, const unsigned (&a0)[4], const unsigned (&a1)[4], const f32x4 (&acc)[4][4]) {
    const int fg = lane >> 4, l15 = lane & 15;
    const float qs = br ? QSCALE_B : LOG2E, ks = br ? 1.0f : LOG2E;
#pragma unroll
    for (int n = 0; n < WT<V>::NC; ++n) {
        const int T = WT<V>::T0 + n;
        if (T <= 8) {
            const float sc = T < 4 ? qs : (T > 4 ? ks : (fg < 2 ? qs : ks));
#pragma unroll
            for (int tt = 0; tt < 4; ++tt) {
                const unsigned lo = pack2(acc[tt][n][0] * sc, acc[tt][n][1] * sc), hi = pack2(acc[tt][n][2] * sc, acc[tt][n][3] * sc);
                if (T & 1) { *(LAS unsigned*)(lds + a0[n] + tt * 768) = lo; *(LAS unsigned*)(lds + a1[n] + tt * 768) = hi; }
                else *(LAS u32x2*)(lds + a0[n] + tt * 768) = u32x2{lo, hi};
            }
        } else if (T < 13 || l15 < 8) {
#pragma unroll
            for (int tt = 0; tt < 4; ++tt)
                *(LAS u32x2*)(lds + a0[3] + (T - 9) * 16 * VROW + tt * 32) = u32x2{pack2(acc[tt][n][0], acc[tt][n][1]), pack2(acc[tt][n][2], acc[tt][n][3])};
        }
    }
    if (V == 3 && l15 == 8) {
#pragma unroll
        for (int tt = 0; tt < 4; ++tt) *(LAS f32x4*)(lds + L_G + (64 * (w & 1) + 16 * tt + 4 * fg) * 4) = acc[tt][2];
    }
}
__device__ __forceinline__ float gate_at(const LAS char* lds, int tok) {
    return __builtin_amdgcn_rcpf(1.0f + __builtin_amdgcn_exp2f(-LOG2E * *(const LAS float*)(lds + L_G + tok * 4)));
}

__device__ __forceinline__ void write_o(LAS char* ob, int lane, int head, int tok0, const f32x16& o, float scale) {
    const int h = lane >> 5, tok = tok0 + (lane & 31);
    LAS char* a = ob + tok * 144 + head * 36 + 8 * h;
    *(LAS unsigned*)(a) = pack2(o[0] * scale, o[1] * scale);
    *(LAS unsigned*)(a + 4) = pack2(o[2] * scale, o[3] * scale);
    *(LAS unsigned*)(a + 16) = pack2(o[4] * scale, o[5] * scale);
    *(LAS unsigned*)(a + 20) = pack2(o[6] * scale, o[7] * scale);
    if (h == 0) *(LAS unsigned*)(a + 32) = pack2(o[8] * scale, o[9] * scale);
}

template <bool USEMAX>
__device__ __forceinline__ f32x16 attn_item(const LAS char* kp, const LAS char* vp, bf16x8 q0, bf16x4 q1, int kb0, int kbd, bool tail, int lane) {
    const int r = lane & 31, h = lane >> 5;
    float m = 0.f;
    if (USEMAX) {
        m = NEG;
        for (int kb = kb0; kb <= kbd; ++kb) {
            bf16x8 k0 = *(const LAS bf16x8*)(kp + kb * 1536);
            bf16x4 k1 = {0, 0, 0, 0}; { unsigned t = *(const LAS unsigned*)(kp + kb * 1536 + 32 - 16 * h); k1[0] = (short)(t & 0xffff); k1[1] = (short)(t >> 16); }
            f32x16 s = {}; s = mfma32(k0, q0, s); s = mfma32k8(k1, q1, s);
#pragma unroll
            for (int i = 0; i < 16; ++i) { const int c = (i & 3) + 8 * (i >> 2) + 4 * h;
                const bool ok = (kb == kbd) ? (c <= r) : ((tail && kb == kb0) ? (c > r) : true); m = fmaxf(m, ok ? s[i] : NEG); }
        }
        m = swap_max(m);
    }
    f32x16 o = {};
    for (int kb = kb0; kb <= kbd; ++kb) {
        bf16x8 k0 = *(const LAS bf16x8*)(kp + kb * 1536);
        bf16x4 k1 = {0, 0, 0, 0}; { unsigned t = *(const LAS unsigned*)(kp + kb * 1536 + 32 - 16 * h); k1[0] = (short)(t & 0xffff); k1[1] = (short)(t >> 16); }
        f32x16 s;
#pragma unroll
        for (int i = 0; i < 16; ++i) s[i] = -m;
        if (kb == kbd) {
#pragma unroll
            for (int i = 0; i < 16; ++i) { const int c = (i & 3) + 8 * (i >> 2) + 4 * h; s[i] = (c <= r) ? -m : NEG; }
        } else if (tail && kb == kb0) {
#pragma unroll
            for (int i = 0; i < 16; ++i) { const int c = (i & 3) + 8 * (i >> 2) + 4 * h; s[i] = (c > r) ? -m : NEG; }
        }
        s = mfma32(k0, q0, s); s = mfma32k8(k1, q1, s);
        u32x4 p0, p1;
#pragma unroll
        for (int i = 0; i < 4; ++i) {
            p0[i] = pack2(__builtin_amdgcn_exp2f(s[2 * i]), __builtin_amdgcn_exp2f(s[2 * i + 1]));
            p1[i] = pack2(__builtin_amdgcn_exp2f(s[8 + 2 * i]), __builtin_amdgcn_exp2f(s[9 + 2 * i]));
        }
        bf16x8 v0 = *(const LAS bf16x8*)(vp + kb * 64), v1 = *(const LAS bf16x8*)(vp + kb * 64 + 32);
        o = mfma32(v0, __builtin_bit_cast(bf16x8, p0), o);
        o = mfma32(v1, __builtin_bit_cast(bf16x8, p1), o);
    }
    return o;
}

enum { M_NONE, M_ZERO, M_DIAG, M_TAIL };
struct AttnState { f32x16 oA, oB, sA, sB, mD; bf16x8 qA0, qB0; bf16x4 qA1, qB1; const LAS char* kp; const LAS char* k1p; const LAS char* vp; };
__device__ __forceinline__ void exp_pack(const f32x16& s, bf16x8& p0, bf16x8& p1) {
    u32x4 a, b;
#pragma unroll
    for (int i = 0; i < 4; ++i) {
        a[i] = pack2(__builtin_amdgcn_exp2f(s[2 * i]), __builtin_amdgcn_exp2f(s[2 * i + 1]));
        b[i] = pack2(__builtin_amdgcn_exp2f(s[8 + 2 * i]), __builtin_amdgcn_exp2f(s[9 + 2 * i]));
    }
    p0 = __builtin_bit_cast(bf16x8, a); p1 = __builtin_bit_cast(bf16x8, b);
}
template <int M>
__device__ __forceinline__ f32x16 score(const AttnState& st, bf16x8 k0, bf16x4 k1, bf16x8 q0, bf16x4 q1) {
    f32x16 c = {};
    if (M == M_DIAG || M == M_TAIL) c = st.mD;
    return mfma32k8(k1, q1, mfma32(k0, q0, c));
}
template <int NA, int NB>
__device__ __forceinline__ void attn_scores(AttnState& st, f32x16& nA, f32x16& nB) {
    const bf16x8 k0 = *(const LAS bf16x8*)(st.kp);
    const bf16x4 k1 = __builtin_bit_cast(bf16x4, u32x2{*(const LAS unsigned*)(st.k1p), 0u});
    if (NA != M_NONE) nA = score<NA>(st, k0, k1, st.qA0, st.qA1);
    if (NB != M_NONE) nB = score<NB>(st, k0, k1, st.qB0, st.qB1);
    st.kp -= 1536; st.k1p -= 1536;
}
template <int NA, int NB>
__device__ __forceinline__ void attn_first(AttnState& st) { attn_scores<NA, NB>(st, st.sA, st.sB); }
template <bool A, bool B, int NA, int NB>
__device__ __forceinline__ void attn_step(AttnState& st) {
    const bf16x8 v0 = *(const LAS bf16x8*)(st.vp), v1 = *(const LAS bf16x8*)(st.vp + 32);
    st.vp -= 64;
    f32x16 nA, nB;
    if (NA != M_NONE || NB != M_NONE) attn_scores<NA, NB>(st, nA, nB);
    bf16x8 p0, p1;
    if (A) { exp_pack(st.sA, p0, p1); st.oA = mfma32(v0, p0, st.oA); st.oA = mfma32(v1, p1, st.oA); }
    if (B) { exp_pack(st.sB, p0, p1); st.oB = mfma32(v0, p0, st.oB); st.oB = mfma32(v1, p1, st.oB); }
    if (NA != M_NONE) st.sA = nA;
    if (NB != M_NONE) st.sB = nB;
    __builtin_amdgcn_sched_group_barrier(0x100, 4, 0);
#define SGA __builtin_amdgcn_sched_group_barrier(0x400, 4, 0); __builtin_amdgcn_sched_group_barrier(0x002, 2, 0); __builtin_amdgcn_sched_group_barrier(0x008, 1, 0);
    SGA SGA SGA SGA SGA SGA SGA SGA
#undef SGA
    __builtin_amdgcn_sched_barrier(0);
}

__device__ __forceinline__ void features(const LAS char* rowp, int lane, bf16x8 wf0, bf16x4 wf1, unsigned (&pk)[9]) {
    const int h = lane >> 5;
    bf16x8 b0 = *(const LAS bf16x8*)(rowp + 16 * h);
    unsigned t = *(const LAS unsigned*)(rowp + 32); if (h) t = 0;
    bf16x4 b1 = {(short)(t & 0xffff), (short)(t >> 16), 0, 0};
    f32x16 d = {}; d = mfma32(wf0, b0, d); d = mfma32k8(wf1, b1, d);
    float mx = d[0], mn = d[0];
#pragma unroll
    for (int i = 1; i < 9; ++i) { mx = fmaxf(mx, d[i]); mn = fminf(mn, d[i]); }
    mx = swap_max(mx); mn = swap_min(mn);
    float ep[9], en[9], sp = 0.f, sn = 0.f;
#pragma unroll
    for (int i = 0; i < 9; ++i) { ep[i] = __builtin_amdgcn_exp2f(d[i] - mx); en[i] = __builtin_amdgcn_exp2f(mn - d[i]); sp += ep[i]; sn += en[i]; }
    sp = __builtin_amdgcn_rcpf(swap_sum(sp)); sn = __builtin_amdgcn_rcpf(swap_sum(sn));
    float v[18];
#pragma unroll
    for (int i = 0; i < 9; ++i) { v[i] = ep[i] * sp; v[9 + i] = en[i] * sn; }
#pragma unroll
    for (int i = 0; i < 9; ++i) pk[i] = pack2(v[2 * i], v[2 * i + 1]);
}

constexpr int P3_GB = 80 * 144, P3_W0 = 4 * P3_GB, P3_WQ = 36864;
__device__ __forceinline__ void phase3(const Params& p, LAS char* lds, int tid, int lane, int w, int t0, int ct, u64 tag) {
    const unsigned lbase = (unsigned)(size_t)lds;
    const __amdgpu_buffer_rsrc_t ro = __builtin_amdgcn_make_buffer_rsrc((void*)(p.ws + WS_O), 0, 16 * 2048 * 144, 0x00020000);
    const __amdgpu_buffer_rsrc_t rwa = __builtin_amdgcn_make_buffer_rsrc((void*)p.woA, 0, D * D * 2, 0x00020000);
    const __amdgpu_buffer_rsrc_t rwb = __builtin_amdgcn_make_buffer_rsrc((void*)p.woB, 0, D * D * 2, 0x00020000);
    const int wrow = lane >> 3;
    const unsigned vwo0 = wrow * 1152 + ct * 128 + (((lane & 7) ^ (((wrow >> 1) & 1) << 1)) << 4), vwo1 = vwo0 ^ 64;
    auto issue_w = [&](int q) {
        if (w < 9) {
            const unsigned lb = lbase + P3_W0 + (q & 1) * P3_WQ + w * 4096, so = ((q & 1) * 288 + 32 * w) * 1152;
#pragma unroll
            for (int j = 0; j < 4; ++j) glds16(lb + j * 1024, (j & 1) ? vwo1 : vwo0, q < 2 ? rwa : rwb, so + j * 8 * 1152);
        }
    };
    issue_w(0); issue_w(1);
    const int rb0 = t0 >> 7, rb1 = (t0 + 79 < S ? t0 + 79 : S - 1) >> 7;
    const char* fl = p.ws + WS_FO + (((lane & 8) ? rb1 : rb0) * 16 + (lane & 7)) * 128;
    unsigned oo[5], ol[5];
#pragma unroll
    for (int j = 0; j < 5; ++j) { const int c = tid + 640 * j, sl = (c * 5826) >> 22, wi = c - 720 * sl;
        oo[j] = c < 2880 ? (sl * 2048 + t0) * 144 + wi * 16 : 0xC0000000u;
        ol[j] = c < 2880 ? c * 16 : P3_W0 + 2 * P3_WQ + lane * 16; }
    const int fg = lane >> 4, l15 = lane & 15;
    const int tt = w >= 5 ? w - 5 : w, cp = w >= 5;
    const int fl2 = ((l15 >> 3) & 1) | ((fg & 1) << 1);
    const unsigned wl0 = P3_W0 + (72 * fg + (l15 >> 2)) * 128 + (((2 * cp) ^ fl2) << 5) + 8 * (lane & 3);
    const unsigned ofb = fg * P3_GB + (16 * tt + l15) * 144;
    f32x4 acc0 = {0, 0, 0, 0}, acc1 = {0, 0, 0, 0};
    u32x4 ra[5], rb_[5];
    auto load_o = [&](u32x4 (&r)[5], int q) {
#pragma unroll
        for (int j = 0; j < 5; ++j) r[j] = __builtin_amdgcn_raw_buffer_load_b128(ro, oo[j], q * (4 * 2048 * 144), 16);
        __builtin_amdgcn_sched_barrier(0);
    };
    auto write_ob = [&](const u32x4 (&r)[5]) {
#pragma unroll
        for (int j = 0; j < 5; ++j) *(LAS u32x4*)(lds + ol[j]) = r[j];
    };
    auto compute = [&](int qodd) {
        const unsigned we = wl0 + qodd * P3_WQ, wo_ = we ^ 64;
        bf16x8 w0[9], w1[9], o8[9];
#pragma unroll
        for (int j = 0; j < 9; ++j) { const unsigned wa = ((j & 1) ? wo_ : we) + j * 1024;
            w0[j] = cat(trread(lds + wa), trread(lds + wa + 512));
            w1[j] = cat(trread(lds + (wa ^ 32)), trread(lds + (wa ^ 32) + 512));
            o8[j] = *(const LAS bf16x8*)(lds + ofb + j * 16); }
#pragma unroll
        for (int j = 0; j < 9; ++j) { acc0 = mfma16(w0[j], o8[j], acc0); acc1 = mfma16(w1[j], o8[j], acc1); }
    };
#pragma unroll 1
    for (int hf = 0; hf < 2; ++hf) {
        if (w == 9) wait_flags(fl + hf * (8 * 128), lane < 16, tag, p.ws);
        BAR();
        STAMP(16 + 3 * hf);
        load_o(ra, 2 * hf); load_o(rb_, 2 * hf + 1);
        write_ob(ra);
        BAR();
        STAMP(17 + 3 * hf);
        compute(0);
        BAR();
        write_ob(rb_);
        if (hf == 0) issue_w(2);
        BAR();
        STAMP(18 + 3 * hf);
        compute(1);
        if (hf == 0) { BAR(); issue_w(3); }
    }
    STAMP(22);
    const int trow = t0 + 16 * tt + l15;
    if (trow < S) {
        char* dst = (char*)p.out + (size_t)trow * 1152 + ct * 128 + cp * 64 + fg * 8;
        *(u32x2*)(dst) = u32x2{pack2(acc0[0], acc0[1]), pack2(acc0[2], acc0[3])};
        *(u32x2*)(dst + 32) = u32x2{pack2(acc1[0], acc1[1]), pack2(acc1[2], acc1[3])};
    }
}

__global__ __launch_bounds__(640, 3) void k_fused(Params p) {
    extern __shared__ __attribute__((aligned(16))) char smem[];
    LAS char* lds = (LAS char*)smem;
    const int tid = threadIdx.x, lane = tid & 63, w = __builtin_amdgcn_readfirstlane(tid >> 6);
    const int bid = blockIdx.x, g = bid & 7, idx = bid >> 3, br = idx & 1, rb = idx >> 1, r0 = rb * 128;
    const int s0 = (rb * 5 + g * 2 + br * 4) % 9;
    STAMP_INIT();
    STAMP(0);
    u64 tag;
    const __amdgpu_buffer_rsrc_t rws = __builtin_amdgcn_make_buffer_rsrc((void*)p.ws, 0, 16 << 20, 0x00020000);
    if (w >= 8) {
        if (w == 8 && lane == 0) *(LAS unsigned*)(lds + L_G + 512) = 0u;
        tag = (w == 8) ? load_loop<0>(p, lds, lane, g, br, r0, s0) : load_loop<1>(p, lds, lane, g, br, r0, s0);
        BAR();
        if (w == 8 && lane < 49) *(LAS u32x4*)(lds + L_V + 72 * VROW + lane * 16) = u32x4{0x3F803F80u, 0x3F803F80u, 0x3F803F80u, 0x3F803F80u};
    } else {
        tag = *(const __attribute__((address_space(4))) u64*)(p.ws + WS_EPOCH) + 1;
        asm volatile("" :: "s"(tag));
        f32x4 acc[4][4];
#pragma unroll
        for (int t = 0; t < 4; ++t)
#pragma unroll
            for (int n = 0; n < 4; ++n) acc[t][n] = f32x4{0, 0, 0, 0};
        unsigned a0[4], a1[4];
#define RUN(V) do { image_addr<V>(w, lane, a0, a1); gemm_loop<V>(lds, w, lane, s0, acc); STAMP(1); BAR(); write_images<V>(lds, w, lane, br, a0, a1, acc); } while (0)
        const int v = ((w >> 1) & 1) * 2 + (w >> 2);
        if (v == 0) RUN(0); else if (v == 1) RUN(1); else if (v == 2) RUN(2); else RUN(3);
#undef RUN
    }
    BAR();
    STAMP(2);
    const int head = w & 3, h = lane >> 5, r31 = lane & 31;
    LAS char* ob = lds + L_Q;
    if (br) {
        if (rb < 15) {
            const unsigned kg = (unsigned)WS_KG + (rb * 8 + g) * KG_BYTES;
#pragma unroll
            for (int j = 0; j < 5; ++j) { const int c = tid + 640 * j;
                if (j < 4 || c < 2688) {
                    int a;
                    if (j < 2 || (j == 2 && c < 1536)) { const int hd = (c * 683) >> 18, wi = c - 384 * hd; a = L_K + hd * 18432 + 12288 + wi * 16; }
                    else { const int cc = c - 1536; a = L_V + (cc >> 4) * VROW + 512 + (cc & 15) * 16; }
                    __builtin_amdgcn_raw_buffer_store_b128(*(const LAS u32x4*)(lds + a), rws, kg + c * 16, 0, 16);
                } }
        }
#define KV_ARRIVE() do { WAITV(0); unsigned prev = 0; \
            if (lane == 0) prev = __hip_atomic_fetch_add((LAS unsigned*)(lds + L_G + 512), 1u, __ATOMIC_RELAXED, __HIP_MEMORY_SCOPE_WORKGROUP); \
            if (lane == 0 && prev == 9) set_flag(p.ws + WS_FKV + (rb * 8 + g) * 128, tag); } while (0)
        if (w >= 8) {
            const int t2 = tid - 512;
            KV_ARRIVE();
            if (w == 8) STAMPW(11, p.ws);
            if (rb > 0) wait_flags(p.ws + WS_FKV + ((rb - 1 - (lane & 1)) * 8 + g) * 128, lane < 2 && rb - 1 - lane >= 0, tag, p.ws);
            if (w == 8) STAMPW(12, p.ws);
#pragma unroll
            for (int b = 0; b < 2; ++b) {
                u32x4 v[21];
                if (rb > b) {
                    const unsigned src = (unsigned)WS_KG + ((rb - 1 - b) * 8 + g) * KG_BYTES + t2 * 16;
#pragma unroll
                    for (int j = 0; j < 21; ++j) v[j] = __builtin_amdgcn_raw_buffer_load_b128(rws, src + j * 2048, 0, 16);
                    __builtin_amdgcn_sched_barrier(0);
#pragma unroll
                    for (int j = 0; j < 12; ++j) { const int c = t2 + 128 * j, hd = j / 3, wi = c - 384 * hd;
                        *(LAS u32x4*)(lds + L_K + hd * 18432 + (1 - b) * 6144 + wi * 16) = v[j]; }
#pragma unroll
                    for (int j = 0; j < 9; ++j) { const int c = t2 + 128 * j;
                        *(LAS u32x4*)(lds + L_V + (c >> 4) * VROW + (1 - b) * 256 + (c & 15) * 16) = v[12 + j]; }
                }
                if (w == 8) STAMPW(13 + b, p.ws);
                BAR();
            }
        } else {
            AttnState st;
            const int j0 = w >> 2;
            { const LAS char* qp = lds + L_Q + head * 6144 + (32 * j0 + r31) * 48;
              st.qA0 = *(const LAS bf16x8*)(qp + 16 * h); st.qB0 = *(const LAS bf16x8*)(qp + 64 * 48 + 16 * h);
              unsigned ta = *(const LAS unsigned*)(qp + 32), tb = *(const LAS unsigned*)(qp + 64 * 48 + 32);
              st.qA1 = __builtin_bit_cast(bf16x4, u32x2{h ? 0u : ta, 0u}); st.qB1 = __builtin_bit_cast(bf16x4, u32x2{h ? 0u : tb, 0u}); }
            const int rr = r31 - 4 * h;
#pragma unroll
            for (int i = 0; i < 16; ++i) st.mD[i] = ((i & 3) + 8 * (i >> 2) <= rr) ? 0.f : NEG;
            st.oA = f32x16{}; st.oB = f32x16{};
            st.kp = lds + L_K + head * 18432 + r31 * 48 + 16 * h + (j0 + 10) * 1536;
            st.k1p = lds + L_K + head * 18432 + r31 * 48 + 32 + (j0 + 10) * 1536;
            st.vp = lds + L_V + (r31 < 18 ? head * 18 + r31 : 72) * VROW + 16 * h + (j0 + 10) * 64;
            attn_first<M_NONE, M_DIAG>(st);
            attn_step<false, true, M_NONE, M_ZERO>(st);
            attn_step<false, true, M_DIAG, M_ZERO>(st);
            KV_ARRIVE();
            if (j0) { attn_step<true, true, M_ZERO, M_ZERO>(st); }
            attn_step<true, true, M_NONE, M_NONE>(st);
            STAMP(3);
            BAR();
            STAMP(4);
            if (rb >= 1) {
                attn_first<M_ZERO, M_ZERO>(st);
                attn_step<true, true, M_ZERO, M_ZERO>(st);
                attn_step<true, true, M_ZERO, M_ZERO>(st);
                attn_step<true, true, M_ZERO, M_ZERO>(st);
                attn_step<true, true, M_NONE, M_NONE>(st);
            }
            BAR();
            STAMP(5);
            if (rb >= 2) {
                int rr2 = rr; asm volatile("" : "+v"(rr2));
#pragma unroll
                for (int i = 0; i < 16; ++i) st.mD[i] = ((i & 3) + 8 * (i >> 2) <= rr2) ? NEG : 0.f;
                if (j0 == 0) { attn_first<M_ZERO, M_ZERO>(st); attn_step<true, true, M_ZERO, M_TAIL>(st); }
                else attn_first<M_ZERO, M_TAIL>(st);
                attn_step<true, true, M_ZERO, M_NONE>(st);
                attn_step<true, false, M_TAIL, M_NONE>(st);
                attn_step<true, false, M_NONE, M_NONE>(st);
            }
            const LAS char* kpb = lds + L_K + head * 18432 + r31 * 48 + 16 * h;
            const LAS char* vpb = lds + L_V + (r31 < 18 ? head * 18 + r31 : 72) * VROW + 16 * h;
#pragma unroll
            for (int it = 0; it < 2; ++it) {
                const int j = j0 + 2 * it;
                f32x16 o = it ? st.oB : st.oA;
                float l = swap_lo(o[10]);
                if (__any(!(l >= 9.3e-10f && l <= 1.07e9f))) {
                    const int J = 4 * rb + j, nb = J < 8 ? J : 8;
                    o = attn_item<true>(kpb, vpb, it ? st.qB0 : st.qA0, it ? st.qB1 : st.qA1, 8 + j - nb, 8 + j, J >= 8, lane);
                    l = swap_lo(o[10]);
                }
                const float gt = gate_at(lds, 32 * j + r31);
                write_o(ob, lane, head, 32 * j, o, (1.0f - gt) * __builtin_amdgcn_rcpf(l));
            }
        }
    } else {
        const int fg = lane >> 4, l15 = lane & 15;
        LAS char* kvb = lds + L_V;
        unsigned pq[4][9];
        if (w < 8) {
            bf16x8 wf0; bf16x4 wf1;
            { const int hh = (r31 >> 2) & 1, i = (r31 & 3) + 4 * (r31 >> 3); const bool valid = i <= 8 && r31 < 24; const int f = valid ? 9 * hh + i : 0;
              const int rot = (head & 1) ? 2 : 0; const LAS unsigned short* wp = (const LAS unsigned short*)(lds + L_WPHI) + head * 324 + f;
#pragma unroll
              for (int j = 0; j < 8; ++j) { unsigned short v = wp[(8 * h + j + rot) * 18]; wf0[j] = valid ? (short)v : (short)0; }
              const int d16 = (head & 1) ? 0 : 16;
              unsigned short a = wp[d16 * 18], b = wp[(d16 + 1) * 18];
              wf1 = (valid && h == 0) ? bf16x4{(short)a, (short)b, 0, 0} : bf16x4{0, 0, 0, 0}; }
            if (w < 4) {
                __builtin_amdgcn_s_setprio(1);
                LAS char* pt = lds + L_K + head * 18432;
                f32x4 kv[3][2];
#pragma unroll
                for (int a = 0; a < 3; ++a) { kv[a][0] = f32x4{0, 0, 0, 0}; kv[a][1] = f32x4{0, 0, 0, 0}; }
                const LAS char* tr = pt + (16 * (fg >> 1) + 4 * (fg & 1) + (l15 >> 2)) * 80 + 8 * (lane & 3);
                const LAS char* vb0p = lds + L_V + (head * 18 + l15) * VROW + (256 + 8 * fg) * 2;
                const LAS char* vb1p = lds + L_V + (l15 < 2 ? head * 18 + 16 + l15 : 72) * VROW + (256 + 8 * fg) * 2;
                unsigned pk[4][9];
#pragma unroll
                for (int tb = 0; tb < 4; ++tb) features(lds + L_K + head * 18432 + (256 + 32 * tb + r31) * 48, lane, wf0, wf1, pk[tb]);
#pragma unroll
                for (int tb = 0; tb < 4; ++tb) {
                    LAS char* wr = pt + tb * 2560 + r31 * 80 + 40 * h;
                    *(LAS u32x2*)(wr) = u32x2{pk[tb][0], pk[tb][1]}; *(LAS u32x2*)(wr + 8) = u32x2{pk[tb][2], pk[tb][3]}; *(LAS u32x2*)(wr + 16) = u32x2{pk[tb][4], pk[tb][5]};
                    *(LAS u32x2*)(wr + 24) = u32x2{pk[tb][6], pk[tb][7]}; *(LAS unsigned*)(wr + 32) = pk[tb][8];
                }
#pragma unroll
                for (int tb = 0; tb < 4; ++tb) {
                    const bf16x8 vb0 = *(const LAS bf16x8*)(vb0p + tb * 64), vb1 = *(const LAS bf16x8*)(vb1p + tb * 64);
#pragma unroll
                    for (int ft = 0; ft < 3; ++ft) {
                        const bf16x8 af = cat(trread(tr + tb * 2560 + ft * 32), trread(tr + tb * 2560 + ft * 32 + 8 * 80));
                        kv[ft][0] = mfma16(af, vb0, kv[ft][0]); kv[ft][1] = mfma16(af, vb1, kv[ft][1]);
                    }
                }
                LAS char* kr = pt + 10240;
#pragma unroll
                for (int ft = 0; ft < 3; ++ft)
#pragma unroll
                    for (int dt = 0; dt < 2; ++dt) { const int drow = 16 * dt + l15, f = 16 * ft + 4 * fg;
                        if (drow < 19 && f < 40) *(LAS u32x2*)(kr + drow * 80 + f * 2) = u32x2{pack2(kv[ft][dt][0], kv[ft][dt][1]), pack2(kv[ft][dt][2], kv[ft][dt][3])}; }
                const unsigned kvp = (unsigned)WS_KVP + (g * 16 + rb) * KVP_BYTES + head * 1520;
#pragma unroll
                for (int j = 0; j < 2; ++j) { const int c = lane + 64 * j;
                    if (c < 95) __builtin_amdgcn_raw_buffer_store_b128(*(const LAS u32x4*)(kr + c * 16), rws, kvp + c * 16, 0, 16); }
                WAITV(0);
                unsigned prev = 0;
                if (lane == 0) prev = __hip_atomic_fetch_add((LAS unsigned*)(lds + L_G + 512), 1u, __ATOMIC_RELAXED, __HIP_MEMORY_SCOPE_WORKGROUP);
                if (lane == 0 && prev == 3) set_flag(p.ws + WS_FKP + (rb * 8 + g) * 128, tag);
                __builtin_amdgcn_s_setprio(0);
                STAMP(3);
            } else {
#pragma unroll
                for (int tb = 0; tb < 4; ++tb) features(lds + L_Q + head * 6144 + (32 * tb + r31) * 48, lane, wf0, wf1, pq[tb]);
                STAMP(3);
            }
        } else if (w == 8) wait_flags(p.ws + WS_FKP + ((lane & 15) * 8 + g) * 128, lane < 16, tag, p.ws);
        BAR();
        STAMP(4);
        if (tid < KVP_CHUNKS) {
            u32x4 v[16];
#pragma unroll
            for (int i = 0; i < 16; ++i) v[i] = __builtin_amdgcn_raw_buffer_load_b128(rws, (unsigned)WS_KVP + (g * 16 + i) * KVP_BYTES + tid * 16, 0, 16);
            float sum[8];
#pragma unroll
            for (int e = 0; e < 8; ++e) sum[e] = 0.f;
#pragma unroll
            for (int i = 0; i < 16; ++i)
#pragma unroll
                for (int e = 0; e < 4; ++e) { sum[2 * e] += __uint_as_float(v[i][e] << 16); sum[2 * e + 1] += __uint_as_float(v[i][e] & 0xffff0000u); }
            const int row = (tid * 13108) >> 16, f = (tid - 5 * row) * 8;
            LAS char* dst = kvb + (row / 5) * VROW + (row % 5) * 96;
            *(LAS u32x2*)(dst + (f >= 20 ? 8 + 2 * f : 2 * f)) = u32x2{pack2(sum[0], sum[1]), pack2(sum[2], sum[3])};
            *(LAS u32x2*)(dst + (f + 4 >= 20 ? 16 + 2 * f : 8 + 2 * f)) = u32x2{pack2(sum[4], sum[5]), pack2(sum[6], sum[7])};
        }
        BAR();
        STAMP(5);
        if (w >= 4 && w < 8) {
            const int row = head * 19 + (r31 < 19 ? r31 : 18);
            const LAS char* ap = kvb + (row / 5) * VROW + (row % 5) * 96 + 48 * h;
            const bf16x8 a0 = *(const LAS bf16x8*)(ap), a1 = *(const LAS bf16x8*)(ap + 16);
            const bf16x4 a2 = __builtin_bit_cast(bf16x4, u32x2{*(const LAS unsigned*)(ap + 32), 0u});
#pragma unroll
            for (int tb = 0; tb < 4; ++tb) {
                f32x16 o = {};
                o = mfma32(a0, __builtin_bit_cast(bf16x8, u32x4{pq[tb][0], pq[tb][1], pq[tb][2], pq[tb][3]}), o);
                o = mfma32(a1, __builtin_bit_cast(bf16x8, u32x4{pq[tb][4], pq[tb][5], pq[tb][6], pq[tb][7]}), o);
                o = mfma32k8(a2, __builtin_bit_cast(bf16x4, u32x2{pq[tb][8], 0u}), o);
                const float den = swap_lo(o[10]) + 1e-6f;
                write_o(ob, lane, head, 32 * tb, o, gate_at(lds, 32 * tb + r31) * __builtin_amdgcn_rcpf(den));
            }
        }
    }
    BAR();
    STAMP(6);
    { const unsigned od = (unsigned)WS_O + ((br * 8 + g) * 2048 + r0) * 144;
#pragma unroll
      for (int j = 0; j < 2; ++j) { const int c = tid + 640 * j;
          if (c < 1152) __builtin_amdgcn_raw_buffer_store_b128(*(const LAS u32x4*)(ob + c * 16), rws, od + c * 16, 0, 16); } }
    WAITV(0);
    BAR();
    if (tid == 0) set_flag(p.ws + WS_FO + (rb * 16 + br * 8 + g) * 128, tag);
    STAMP(7);
    if (bid == 255 && w == 8) {
        for (unsigned spins = 0;;) {
            bool ok = true;
#pragma unroll
            for (int j = 0; j < 4; ++j) ok &= __hip_atomic_load((const GAS u64*)(p.ws + (j < 2 ? WS_FKV : WS_FKP - 16384) + (lane + 64 * j) * 128), __ATOMIC_RELAXED, __HIP_MEMORY_SCOPE_AGENT) == tag;
            if (__all(ok)) { if (lane == 0) set_flag(p.ws + WS_EPOCH, tag); break; }
            if (++spins > SPIN_CAP) break;
            __builtin_amdgcn_s_sleep(1);
        }
    }
    { const int xcd = bid & 7, ii = bid >> 3;
      if (ii < 27) phase3(p, lds, tid, lane, w, (xcd * 3 + ii / 9) * 80, ii % 9, tag);
      else { const int sp = xcd * 5 + ii - 27; if (sp < 18) phase3(p, lds, tid, lane, w, (24 + sp / 9) * 80, sp % 9, tag); } }
    STAMP_DUMP();
}

extern "C" void kernel_launch(void* const* d_in, const int* in_sizes, int n_in, void* d_out, int out_size, void* d_ws, size_t ws_size, hipStream_t stream) {
    Params p;
    p.x = d_in[0]; p.wqkvA = d_in[1]; p.wphi = d_in[2]; p.woA = d_in[3]; p.wqkvB = d_in[5]; p.woB = d_in[6]; p.gw = d_in[7];
    p.out = d_out; p.ws = (char*)d_ws;
    hipFuncSetAttribute((const void*)k_fused, hipFuncAttributeMaxDynamicSharedMemorySize, LDS_BYTES);
    k_fused<<<256, 640, LDS_BYTES, stream>>>(p);
}
```

```cpp
#include <hip/hip_runtime.h>
#include <stdint.h>
#include <type_traits>
#define USE_DOT2 0
#define FORCE_FB 0
#ifndef EXIT_AT
#define EXIT_AT 0
#endif
#define NOGEMM 0
#define OUT_AUX 0
#define ROT 1

#define LAS __attribute__((address_space(3)))
#define GAS __attribute__((address_space(1)))
typedef short bf16x8 __attribute__((ext_vector_type(8)));
typedef short bf16x4 __attribute__((ext_vector_type(4)));
typedef float f32x2 __attribute__((ext_vector_type(2)));
typedef float f32x4 __attribute__((ext_vector_type(4)));
typedef float f32x16 __attribute__((ext_vector_type(16)));
typedef unsigned u32x2 __attribute__((ext_vector_type(2)));
typedef unsigned u32x4 __attribute__((ext_vector_type(4)));
typedef __bf16 bf2_t __attribute__((ext_vector_type(2)));
typedef unsigned long long u64;

constexpr int S = 2048, D = 576;
constexpr float LOG2E = 1.4426950408889634f;
constexpr float QSCALE_B = 0.23570226039551584f * LOG2E;
constexpr float NEG = -1.0e30f;

constexpr size_t WS_EPOCH = 0, WS_FKV = 4096, WS_FKP = 20480, WS_FO = 36864, WS_TMO = 69632;
constexpr size_t WS_KG = 131072;
constexpr int KG_BYTES = 43008, KG_K = 24576;
constexpr size_t WS_KVP = 6291456;
constexpr int KVP_BYTES = 6144, KVP_CHUNKS = 380;
constexpr size_t WS_O = 8388608;
constexpr size_t WS_STAMP = 16777216;

constexpr int XS = 16384, WSB = 27648, BUFS = XS + WSB;
constexpr int L_K = 0;
constexpr int L_V = 73728, VROW = 784;
constexpr int L_Q = 130960;
constexpr int L_G = 155648;
constexpr int L_GW = 158720;
constexpr int L_WPHI = 160768;
constexpr int LDS_BYTES = 163840;
constexpr unsigned SPIN_CAP = 1u << 18;

struct Params {
    const void* x; const void* wqkvA; const void* wphi; const void* woA; const void* wqkvB; const void* woB; const void* gw;
    void* out; char* ws;
};

__device__ __forceinline__ unsigned pack2(float a, float b) { f32x2 v = {a, b}; bf2_t r = __builtin_convertvector(v, bf2_t); return __builtin_bit_cast(unsigned, r); }
__device__ __forceinline__ void glds16(unsigned ldsaddr, unsigned voff, __amdgpu_buffer_rsrc_t rsrc, unsigned soff) {
    asm volatile("s_mov_b32 m0, %0\n\ts_nop 0\n\tbuffer_load_dwordx4 %1, %2, %3 offen lds" :: "s"(ldsaddr), "v"(voff), "s"(rsrc), "s"(soff) : "memory");
}
#define BAR() asm volatile("s_waitcnt lgkmcnt(0)\n\ts_barrier" ::: "memory")
#define WAITV(n) asm volatile("s_waitcnt vmcnt(" #n ")" ::: "memory")
__device__ __forceinline__ bf16x4 trread(const LAS char* p) { typedef short v4s __attribute__((ext_vector_type(4))); return __builtin_amdgcn_ds_read_tr16_b64_v4i16((LAS v4s*)p); }
__device__ __forceinline__ bf16x8 cat(bf16x4 a, bf16x4 b) { return __builtin_shufflevector(a, b, 0, 1, 2, 3, 4, 5, 6, 7); }
__device__ __forceinline__ f32x4 mfma16(bf16x8 a, bf16x8 b, f32x4 c) { return __builtin_amdgcn_mfma_f32_16x16x32_bf16(a, b, c, 0, 0, 0); }
__device__ __forceinline__ f32x16 mfma32(bf16x8 a, bf16x8 b, f32x16 c) { return __builtin_amdgcn_mfma_f32_32x32x16_bf16(a, b, c, 0, 0, 0); }
__device__ __forceinline__ f32x16 mfma32k8(bf16x4 a, bf16x4 b, f32x16 c) { return __builtin_amdgcn_mfma_f32_32x32x8bf16_1k(a, b, c, 0, 0, 0); }
__device__ __forceinline__ float swap_lo(float v) {
    auto r = __builtin_amdgcn_permlane32_swap(__float_as_uint(v), __float_as_uint(v), false, false); return __uint_as_float(r[0]); }
__device__ __forceinline__ float swap_max(float v) { auto r = __builtin_amdgcn_permlane32_swap(__float_as_uint(v), __float_as_uint(v), false, false); return fmaxf(__uint_as_float(r[0]), __uint_as_float(r[1])); }
__device__ __forceinline__ float swap_min(float v) { auto r = __builtin_amdgcn_permlane32_swap(__float_as_uint(v), __float_as_uint(v), false, false); return fminf(__uint_as_float(r[0]), __uint_as_float(r[1])); }
__device__ __forceinline__ float swap_sum(float v) { auto r = __builtin_amdgcn_permlane32_swap(__float_as_uint(v), __float_as_uint(v), false, false); return __uint_as_float(r[0]) + __uint_as_float(r[1]); }

__device__ __forceinline__ bool wait_flags(const char* p, bool active, u64 tag, char* ws) {
    for (unsigned spins = 0;;) {
        u64 v = active ? __hip_atomic_load((const GAS u64*)p, __ATOMIC_RELAXED, __HIP_MEMORY_SCOPE_AGENT) : tag;
        if (__all(v == tag)) return true;
        __builtin_amdgcn_s_sleep(1);
        if (++spins > SPIN_CAP) { __hip_atomic_store((GAS unsigned*)(ws + WS_TMO), 1u, __ATOMIC_RELAXED, __HIP_MEMORY_SCOPE_AGENT); return false; }
    }
}
__device__ __forceinline__ void set_flag(char* p, u64 tag) { __hip_atomic_store((GAS u64*)p, tag, __ATOMIC_RELAXED, __HIP_MEMORY_SCOPE_AGENT); }

#define STAMP(i) do {} while (0)
#define STAMP9(i) do {} while (0)
#define STAMPW(i, ws_) do {} while (0)
#define STAMP_DUMP() do {} while (0)
#define STAMP_INIT() do {} while (0)

template <int V> struct WT { static constexpr int T0 = V == 0 ? 0 : V == 1 ? 4 : V == 2 ? 7 : 11, NC = (V & 1) ? 3 : 4; };
constexpr int WSEC = 64 * 144;
template <int V> struct Frags { bf16x8 x[4], w[WT<V>::NC]; };
template <int V>
__device__ __forceinline__ void read_frags(Frags<V>& f, const LAS char* xp, const LAS char* wp, const LAS char* wp4, const LAS char* gp, bool gsel) {
#pragma unroll
    for (int t = 0; t < 4; ++t) f.x[t] = *(const LAS bf16x8*)(xp + t * 2048);
#pragma unroll
    for (int n = 0; n < WT<V>::NC; ++n) {
        constexpr int T0 = WT<V>::T0; const int T = T0 + n, sec = (16 * T) / 72, off = sec * WSEC + (16 * T - 72 * sec) * 2;
        if (T == 4) f.w[n] = cat(trread(wp4), trread(wp4 + 144));
        else f.w[n] = cat(trread(wp + off), trread(wp + off + 144));
    }
    if (V == 3) { const bf16x8 gf = *(const LAS bf16x8*)(gp); const u32x4 a = __builtin_bit_cast(u32x4, f.w[2]), b = __builtin_bit_cast(u32x4, gf);
        f.w[2] = __builtin_bit_cast(bf16x8, u32x4{gsel ? b[0] : a[0], gsel ? b[1] : a[1], gsel ? b[2] : a[2], gsel ? b[3] : a[3]}); }
}
template <int V>
__device__ __forceinline__ void mma_frags(const Frags<V>& f, f32x4 (&acc)[4][4]) {
#pragma unroll
    for (int n = 0; n < WT<V>::NC; ++n)
#pragma unroll
        for (int t = 0; t < 4; ++t) acc[t][n] = (WT<V>::T0 + n <= 8) ? mfma16(f.w[n], f.x[t], acc[t][n]) : mfma16(f.x[t], f.w[n], acc[t][n]);
}
template <int V>
__device__ __forceinline__ void interleave() {
#define SG1 __builtin_amdgcn_sched_group_barrier(0x008, 1, 0); __builtin_amdgcn_sched_group_barrier(0x100, 1, 0);
    SG1 SG1 SG1 SG1 SG1 SG1 SG1 SG1 SG1 SG1
    if (V != 1) { SG1 }
    if (V == 0 || V == 2) { SG1 }
#undef SG1
}
template <int V>
__device__ __forceinline__ void gemm_loop(LAS char* lds, int w, int lane, int s0, f32x4 (&acc)[4][4]) {
    const int fg = lane >> 4, l15 = lane & 15, pp = lane & 3;
    const unsigned sw = (l15 >> 1) & 7;
    const unsigned xb0 = (64 * (w & 1) + l15) * 128 + ((fg ^ sw) << 4), xb1 = xb0 ^ 64;
    const unsigned wb = XS + (8 * fg + 2 * (l15 >> 2)) * 144 + 8 * pp;
    const unsigned wb4 = wb + (pp < 2 ? 128 : WSEC - 16);
    const LAS char* gp0 = lds + L_GW + 16 * fg;
    const LAS char* gp = gp0 + s0 * 128;
    int ks = s0;
    const bool gsel = l15 >= 8;
    unsigned cur = 0;
    Frags<V> f0, f1;
    BAR();
    read_frags<V>(f0, lds + xb0, lds + wb, lds + wb4, gp, gsel);
#pragma unroll 1
    for (int s = 0; s < 9; ++s) {
        read_frags<V>(f1, lds + cur + xb1, lds + cur + wb + 32 * 144, lds + cur + wb4 + 32 * 144, gp + 64, gsel);
        mma_frags<V>(f0, acc);
        interleave<V>();
        __builtin_amdgcn_sched_barrier(0);
        cur = (cur == 2 * BUFS) ? 0 : cur + BUFS; ks = (ks == 8) ? 0 : ks + 1; gp = gp0 + ks * 128;
        if (s < 8) BAR();
        read_frags<V>(f0, lds + cur + xb0, lds + cur + wb, lds + cur + wb4, gp, gsel);
        mma_frags<V>(f1, acc);
        interleave<V>();
        __builtin_amdgcn_sched_barrier(0);
    }
}

template <int LW>
__device__ __forceinline__ u64 load_loop(const Params& p, LAS char* lds, int lane, int g, int br, int r0, int s0) {
    constexpr int NV = LW ? 4 : 5;
    const __amdgpu_buffer_rsrc_t rx = __builtin_amdgcn_make_buffer_rsrc((void*)p.x, 0, S * D * 2, 0x00020000);
    const __amdgpu_buffer_rsrc_t rw = __builtin_amdgcn_make_buffer_rsrc((void*)(br ? p.wqkvB : p.wqkvA), 0, D * 3 * D * 2, 0x00020000);
    const unsigned lbase = (unsigned)(size_t)lds;
    const unsigned vx = (r0 + 8 * LW + (lane >> 3)) * 1152 + (((lane & 7) ^ ((4 * LW + (lane >> 4)) & 7)) << 4);
    auto issue_x = [&](int s, unsigned buf) {
#pragma unroll
        for (int i = 0; i < 8; ++i) glds16(lbase + buf + LW * 1024 + i * 2048, vx, rx, s * 128 + i * (16 * 1152));
    };
    STAMP(8);
    const int s1 = s0 == 8 ? 0 : s0 + 1;
    issue_x(s0, 0);
    unsigned vw[NV];
#pragma unroll
    for (int j = 0; j < NV; ++j) {
        const int ci = 64 * (LW + 2 * j) + lane, rho = (ci * 7282) >> 16, jj = ci - 9 * rho;
        const int k = (rho & ~7) + ((rho & 1) << 2) + ((rho >> 1) & 3);
        vw[j] = k * 3456 + g * 144 + jj * 16;
    }
    auto issue_w = [&](int s, unsigned buf) {
#pragma unroll
        for (int sec = 0; sec < 3; ++sec)
#pragma unroll
            for (int j = 0; j < NV; ++j) glds16(lbase + buf + XS + sec * WSEC + (LW + 2 * j) * 1024, vw[j], rw, s * (64 * 3456) + sec * 1152);
    };
    issue_w(s0, 0);
    if (LW == 0) { const __amdgpu_buffer_rsrc_t rg = __builtin_amdgcn_make_buffer_rsrc((void*)p.gw, 0, D * 2, 0x00020000);
        glds16(lbase + L_GW, lane * 16, rg, 0); glds16(lbase + L_GW + 1024, lane * 16, rg, 1024); }
    else if (!br) { const __amdgpu_buffer_rsrc_t rp = __builtin_amdgcn_make_buffer_rsrc((void*)p.wphi, 0, 32 * 324 * 2, 0x00020000);
#pragma unroll
        for (int i = 0; i < 3; ++i) glds16(lbase + L_WPHI + i * 1024, lane * 16, rp, g * 2592 + i * 1024); }
    issue_x(s1, BUFS); issue_w(s1, BUFS);
    STAMP(9);
    const u64 tag = *(const __attribute__((address_space(4))) u64*)(p.ws + WS_EPOCH) + 1;
    asm volatile("" :: "s"(tag));
    unsigned nxt2 = 2 * BUFS; int k2 = s1 == 8 ? 0 : s1 + 1;
#pragma unroll 1
    for (int s = 0; s < 9; ++s) {
        if (s < 8) { if (LW) WAITV(20); else WAITV(23); } else WAITV(0);
        if (s == 0) STAMP(10);
        STAMP9(s);
        BAR();
        STAMP9(9 + s);
        if (s + 2 < 9) { issue_x(k2, nxt2); issue_w(k2, nxt2); }
        nxt2 = (nxt2 == 2 * BUFS) ? 0 : nxt2 + BUFS; k2 = k2 == 8 ? 0 : k2 + 1;
    }
    return tag;
}

template <int V>
__device__ __forceinline__ void image_addr(int w, int lane, unsigned (&a0)[4], unsigned (&a1)[4]) {
    const int fg = lane >> 4, l15 = lane & 15;
#pragma unroll
    for (int n = 0; n < 4; ++n) { a0[n] = 0; a1[n] = 0; }
#pragma unroll
    for (int n = 0; n < WT<V>::NC; ++n) {
        const int T = WT<V>::T0 + n;
        if (T <= 8) {
            const int G = 4 * T + fg, q4 = (G * 57) >> 9, m = G - 9 * q4, sec = q4 >> 1, hp = q4 & 1;
            const int hstride = sec ? 384 * 48 : 128 * 48;
            const int base = (sec ? L_K + 256 * 48 : L_Q) + 2 * hp * hstride + (64 * (w & 1) + l15) * 48;
            a0[n] = base + (m < 4 ? 8 * m : (m == 4 ? 32 : hstride + 8 * (m - 5)));
            a1[n] = (m == 4) ? base + hstride + 32 : a0[n] + 4;
            asm volatile("" : "+v"(a0[n])); if (T & 1) asm volatile("" : "+v"(a1[n]));
        }
    }
    if (V >= 2) { a0[3] = L_V + l15 * VROW + (256 + 64 * (w & 1) + 4 * (((fg & 1) << 1) | (fg >> 1))) * 2; asm volatile("" : "+v"(a0[3])); }
}
template <int V>
__device__ __forceinline__ void write_images(LAS char* lds, int w, int lane, int br, const unsigned (&a0)[4], const unsigned (&a1)[4], const f32x4 (&acc)[4][4]) {
    const int fg = lane >> 4, l15 = lane & 15;
    const float qs = br ? QSCALE_B : LOG2E, ks = br ? 1.0f : LOG2E;
#pragma unroll
    for (int n = 0; n < WT<V>::NC; ++n) {
        const int T = WT<V>::T0 + n;
        if (T <= 8) {
            const float sc = T < 4 ? qs : (T > 4 ? ks : (fg < 2 ? qs : ks));
#pragma unroll
            for (int tt = 0; tt < 4; ++tt) {
                const unsigned lo = pack2(acc[tt][n][0] * sc, acc[tt][n][1] * sc), hi = pack2(acc[tt][n][2] * sc, acc[tt][n][3] * sc);
                if (T & 1) { *(LAS unsigned*)(lds + a0[n] + tt * 768) = lo; *(LAS unsigned*)(lds + a1[n] + tt * 768) = hi; }
                else *(LAS u32x2*)(lds + a0[n] + tt * 768) = u32x2{lo, hi};
            }
        } else if (T < 13 || l15 < 8) {
#pragma unroll
            for (int tt = 0; tt < 4; ++tt)
                *(LAS u32x2*)(lds + a0[3] + (T - 9) * 16 * VROW + tt * 32) = u32x2{pack2(acc[tt][n][0], acc[tt][n][1]), pack2(acc[tt][n][2], acc[tt][n][3])};
        }
    }
    if (V == 3 && l15 == 8) {
#pragma unroll
        for (int tt = 0; tt < 4; ++tt) *(LAS f32x4*)(lds + L_G + (64 * (w & 1) + 16 * tt + 4 * fg) * 4) = acc[tt][2];
    }
}
__device__ __forceinline__ float gate_at(const LAS char* lds, int tok) {
    return __builtin_amdgcn_rcpf(1.0f + __builtin_amdgcn_exp2f(-LOG2E * *(const LAS float*)(lds + L_G + tok * 4)));
}

__device__ __forceinline__ void write_o(LAS char* ob, int lane, int head, int tok0, const f32x16& o, float scale) {
    const int h = lane >> 5, tok = tok0 + (lane & 31);
    LAS char* a = ob + tok * 144 + head * 36 + 8 * h;
    *(LAS unsigned*)(a) = pack2(o[0] * scale, o[1] * scale);
    *(LAS unsigned*)(a + 4) = pack2(o[2] * scale, o[3] * scale);
    *(LAS unsigned*)(a + 16) = pack2(o[4] * scale, o[5] * scale);
    *(LAS unsigned*)(a + 20) = pack2(o[6] * scale, o[7] * scale);
    if (h == 0) *(LAS unsigned*)(a + 32) = pack2(o[8] * scale, o[9] * scale);
}

enum { M_NONE, M_ZERO, M_DIAG, M_TAIL };
struct AttnState { f32x16 oA, oB, sA, sB, mD; bf16x8 qA0, qB0; bf16x4 qA1, qB1; const LAS char* kp; const LAS char* k1p; const LAS char* vp; };
__device__ __forceinline__ void exp_pack(const f32x16& s, bf16x8& p0, bf16x8& p1) {
    u32x4 a, b;
#pragma unroll
    for (int i = 0; i < 4; ++i) {
        a[i] = pack2(__builtin_amdgcn_exp2f(s[2 * i]), __builtin_amdgcn_exp2f(s[2 * i + 1]));
        b[i] = pack2(__builtin_amdgcn_exp2f(s[8 + 2 * i]), __builtin_amdgcn_exp2f(s[9 + 2 * i]));
    }
    p0 = __builtin_bit_cast(bf16x8, a); p1 = __builtin_bit_cast(bf16x8, b);
}
template <bool TAIL>
__device__ __forceinline__ f32x16 make_mask(float rbig) {
    f32x16 m;
#pragma unroll
    for (int i = 0; i < 16; ++i) { const float t = rbig - (float)((i & 3) + 8 * (i >> 2)) * 1.2676506002282294e30f; m[i] = __builtin_amdgcn_fmed3f(TAIL ? -t : t, 0.f, -3.0e38f); }
    return m;
}
__device__ __forceinline__ f32x16 attn_slow(const LAS char* kp, const LAS char* vp, bf16x8 q0, bf16x4 q1, int kb0, int kbd, bool tail, int lane, float rbig) {
    const int h = lane >> 5;
    float m = -3.0e38f;
    f32x16 o = {};
#pragma unroll 1
    for (int pass = 0; pass < 2; ++pass) {
#pragma unroll 1
        for (int kb = kb0; kb <= kbd; ++kb) {
            const bf16x8 k0 = *(const LAS bf16x8*)(kp + kb * 1536);
            const bf16x4 k1 = __builtin_bit_cast(bf16x4, u32x2{*(const LAS unsigned*)(kp + kb * 1536 + 32 - 16 * h), 0u});
            f32x16 c = {};
            if (kb == kbd) c = make_mask<false>(rbig); else if (tail && kb == kb0) c = make_mask<true>(rbig);
            f32x16 sc = mfma32k8(k1, q1, mfma32(k0, q0, c));
            if (pass == 0) {
#pragma unroll
                for (int i = 0; i < 16; ++i) m = fmaxf(m, sc[i]);
            } else {
#pragma unroll
                for (int i = 0; i < 16; ++i) sc[i] -= m;
                bf16x8 p0, p1; exp_pack(sc, p0, p1);
                const bf16x8 v0 = *(const LAS bf16x8*)(vp + kb * 64), v1 = *(const LAS bf16x8*)(vp + kb * 64 + 32);
                o = mfma32(v0, p0, o); o = mfma32(v1, p1, o);
            }
        }
        if (pass == 0) m = swap_max(m);
    }
    return o;
}
template <int KB, int NA, int NB>
__device__ __forceinline__ void attn_scores(const AttnState& st, f32x16& nA, f32x16& nB) {
    const bf16x8 k0 = *(const LAS bf16x8*)(st.kp + KB * 1536);
    const bf16x4 k1 = __builtin_bit_cast(bf16x4, u32x2{*(const LAS unsigned*)(st.k1p + KB * 1536), 0u});
    if (NA != M_NONE) nA = mfma32k8(k1, st.qA1, mfma32(k0, st.qA0, NA == M_ZERO ? f32x16{} : st.mD));
    if (NB != M_NONE) nB = mfma32k8(k1, st.qB1, mfma32(k0, st.qB0, NB == M_ZERO ? f32x16{} : st.mD));
}
template <int KB, int NA, int NB>
__device__ __forceinline__ void attn_first(AttnState& st) { attn_scores<KB, NA, NB>(st, st.sA, st.sB); __builtin_amdgcn_sched_barrier(0); }
template <int KB, bool A, bool B, int NA, int NB>
__device__ __forceinline__ void attn_step(AttnState& st) {
    const bf16x8 v0 = *(const LAS bf16x8*)(st.vp + KB * 64), v1 = *(const LAS bf16x8*)(st.vp + KB * 64 + 32);
    f32x16 nA, nB;
    if (NA != M_NONE || NB != M_NONE) attn_scores<(KB > 0 ? KB - 1 : 0), NA, NB>(st, nA, nB);
    bf16x8 p0, p1;
    if (A) { exp_pack(st.sA, p0, p1); st.oA = mfma32(v0, p0, st.oA); st.oA = mfma32(v1, p1, st.oA); }
    if (B) { exp_pack(st.sB, p0, p1); st.oB = mfma32(v0, p0, st.oB); st.oB = mfma32(v1, p1, st.oB); }
    if (NA != M_NONE) st.sA = nA;
    if (NB != M_NONE) st.sB = nB;
    __builtin_amdgcn_sched_group_barrier(0x100, 4, 0);
#define SGA __builtin_amdgcn_sched_group_barrier(0x400, 4, 0); __builtin_amdgcn_sched_group_barrier(0x002, 2, 0); __builtin_amdgcn_sched_group_barrier(0x008, 1, 0);
    SGA SGA SGA SGA SGA SGA SGA SGA
#undef SGA
    __builtin_amdgcn_sched_barrier(0);
}

#define KV_ARRIVE() do { WAITV(0); unsigned prev = 0; \
            if (lane == 0) prev = __hip_atomic_fetch_add((LAS unsigned*)(lds + L_G + 512), 1u, __ATOMIC_RELAXED, __HIP_MEMORY_SCOPE_WORKGROUP); \
            if (lane == 0 && prev == 5) set_flag(p.ws + WS_FKV + (rb * 8 + g) * 128, tag); } while (0)
template <int J0>
__device__ __forceinline__ void attn_wave(const Params& p, LAS char* lds, int w, int lane, int rb, int g, u64 tag) {
    const int head = w & 3, h = lane >> 5, r31 = lane & 31;
    AttnState st;
    { const LAS char* qp = lds + L_Q + head * 6144 + (32 * J0 + r31) * 48;
      st.qA0 = *(const LAS bf16x8*)(qp + 16 * h); st.qB0 = *(const LAS bf16x8*)(qp + 64 * 48 + 16 * h);
      unsigned ta = *(const LAS unsigned*)(qp + 32), tb = *(const LAS unsigned*)(qp + 64 * 48 + 32);
      st.qA1 = __builtin_bit_cast(bf16x4, u32x2{h ? 0u : ta, 0u}); st.qB1 = __builtin_bit_cast(bf16x4, u32x2{h ? 0u : tb, 0u}); }
    float rbig = ((float)(r31 - 4 * h) + 0.5f) * 1.2676506002282294e30f;
    st.mD = make_mask<false>(rbig);
    st.oA = f32x16{}; st.oB = f32x16{};
    st.kp = lds + L_K + head * 18432 + r31 * 48 + 16 * h;
    st.k1p = lds + L_K + head * 18432 + r31 * 48 + 32;
    st.vp = lds + L_V + (r31 < 18 ? head * 18 + r31 : 72) * VROW + 16 * h;
    attn_first<J0 + 10, M_NONE, M_DIAG>(st);
    attn_step<J0 + 10, false, true, M_NONE, M_ZERO>(st);
    attn_step<J0 + 9, false, true, M_DIAG, M_ZERO>(st);
    if (J0) { attn_step<9, true, true, M_ZERO, M_ZERO>(st); attn_step<8, true, true, M_NONE, M_NONE>(st); }
    else { attn_step<8, true, true, M_NONE, M_NONE>(st); KV_ARRIVE(); }
    STAMP(3);
    if (EXIT_AT == 3) { asm volatile("" :: "v"(st.oA), "v"(st.oB)); return; }
    BAR();
    STAMP(4);
    if (EXIT_AT == 4) { asm volatile("" :: "v"(st.oA), "v"(st.oB)); return; }
    if (rb >= 1) {
        attn_first<7, M_ZERO, M_ZERO>(st);
        attn_step<7, true, true, M_ZERO, M_ZERO>(st);
        attn_step<6, true, true, M_ZERO, M_ZERO>(st);
        attn_step<5, true, true, M_ZERO, M_ZERO>(st);
        attn_step<4, true, true, M_NONE, M_NONE>(st);
    }
    BAR();
    STAMP(5);
    if (EXIT_AT == 5) { asm volatile("" :: "v"(st.oA), "v"(st.oB)); return; }
    if (rb >= 2) {
        asm volatile("" : "+v"(rbig));
        st.mD = make_mask<true>(rbig);
        if (J0 == 0) { attn_first<3, M_ZERO, M_ZERO>(st); attn_step<3, true, true, M_ZERO, M_TAIL>(st); }
        else attn_first<3, M_ZERO, M_TAIL>(st);
        attn_step<J0 + 2, true, true, M_ZERO, M_NONE>(st);
        attn_step<J0 + 1, true, false, M_TAIL, M_NONE>(st);
        attn_step<J0, true, false, M_NONE, M_NONE>(st);
    }
    float lA = swap_lo(st.oA[10]), lB = swap_lo(st.oB[10]);
    if (__builtin_expect(__any(!(lA >= 9.3e-10f && lA <= 1.07e9f && lB >= 9.3e-10f && lB <= 1.07e9f)) || FORCE_FB, 0)) {
#pragma unroll 1
        for (int it = 0; it < 2; ++it) {
            const int j = J0 + 2 * it, J = 4 * rb + j, nb = J < 8 ? J : 8;
            const f32x16 o = attn_slow(st.kp, st.vp, it ? st.qB0 : st.qA0, it ? st.qB1 : st.qA1, 8 + j - nb, 8 + j, J >= 8, lane, rbig);
            if (it) st.oB = o; else st.oA = o;
        }
        lA = swap_lo(st.oA[10]); lB = swap_lo(st.oB[10]);
    }
    const float gA = gate_at(lds, 32 * J0 + r31), gB = gate_at(lds, 32 * J0 + 64 + r31);
    write_o(lds + L_Q, lane, head, 32 * J0, st.oA, (1.0f - gA) * __builtin_amdgcn_rcpf(lA));
    write_o(lds + L_Q, lane, head, 32 * J0 + 64, st.oB, (1.0f - gB) * __builtin_amdgcn_rcpf(lB));
}

__device__ __forceinline__ void features2(const LAS char* rowp, int stride, int lane, bf16x8 wf0, bf16x4 wf1, unsigned (&pk)[2][9]) {
    const int h = lane >> 5;
    f32x16 d[2];
#pragma unroll
    for (int b = 0; b < 2; ++b) {
        const bf16x8 b0 = *(const LAS bf16x8*)(rowp + b * stride + 16 * h);
        const unsigned t = *(const LAS unsigned*)(rowp + b * stride + 32);
        d[b] = mfma32k8(wf1, __builtin_bit_cast(bf16x4, u32x2{h ? 0u : t, 0u}), mfma32(wf0, b0, f32x16{}));
    }
    float ep[2][9], en[2][9], sp[2], sn[2];
#pragma unroll
    for (int i = 0; i < 9; ++i)
#pragma unroll
        for (int b = 0; b < 2; ++b) { ep[b][i] = __builtin_amdgcn_exp2f(d[b][i]); en[b][i] = __builtin_amdgcn_exp2f(-d[b][i]); }
#define SUM9(e) (((e[0] + e[1]) + (e[2] + e[3])) + ((e[4] + e[5]) + (e[6] + e[7])) + e[8])
#pragma unroll
    for (int b = 0; b < 2; ++b) { sp[b] = SUM9(ep[b]); sn[b] = SUM9(en[b]); }
#pragma unroll
    for (int b = 0; b < 2; ++b) { sp[b] = swap_sum(sp[b]); sn[b] = swap_sum(sn[b]); }
    constexpr float BIG = 1.152921504606847e18f;
    if (__builtin_expect(__any(!(sp[0] < BIG && sn[0] < BIG && sp[1] < BIG && sn[1] < BIG)) || FORCE_FB, 0)) {
#pragma unroll
        for (int b = 0; b < 2; ++b) {
            float mx = d[b][0], mn = d[b][0];
#pragma unroll
            for (int i = 1; i < 9; ++i) { mx = fmaxf(mx, d[b][i]); mn = fminf(mn, d[b][i]); }
            mx = swap_max(mx); mn = swap_min(mn);
#pragma unroll
            for (int i = 0; i < 9; ++i) { ep[b][i] = __builtin_amdgcn_exp2f(d[b][i] - mx); en[b][i] = __builtin_amdgcn_exp2f(mn - d[b][i]); }
            sp[b] = swap_sum(SUM9(ep[b])); sn[b] = swap_sum(SUM9(en[b]));
        }
    }
#undef SUM9
#pragma unroll
    for (int b = 0; b < 2; ++b) { sp[b] = __builtin_amdgcn_rcpf(sp[b]); sn[b] = __builtin_amdgcn_rcpf(sn[b]); }
#pragma unroll
    for (int b = 0; b < 2; ++b) {
        float v[18];
#pragma unroll
        for (int i = 0; i < 9; ++i) { v[i] = ep[b][i] * sp[b]; v[9 + i] = en[b][i] * sn[b]; }
#pragma unroll
        for (int i = 0; i < 9; ++i) pk[b][i] = pack2(v[2 * i], v[2 * i + 1]);
    }
}

constexpr int P3_R = 76, P3_GC = P3_R * 9, P3_GB = 11008, P3_OQ = 4 * P3_GB,
              P3_W0 = 2 * P3_OQ, P3_WQ = 36864, P3_END = P3_W0 + 2 * P3_WQ;
constexpr int L_RDY = P3_END;
static_assert(P3_END + 256 <= LDS_BYTES, "phase 3 LDS map");
__device__ __forceinline__ void lds_wait(LAS char* lds, int off) {
    for (unsigned spins = 0; spins < (1u << 22); ++spins) {
        if (__builtin_amdgcn_readfirstlane(__hip_atomic_load((LAS unsigned*)(lds + off), __ATOMIC_RELAXED, __HIP_MEMORY_SCOPE_WORKGROUP))) break;
        __builtin_amdgcn_s_sleep(1);
    }
    asm volatile("" ::: "memory");
}
#define P3EXIT 0
#define LST_DECL
#define LST(i)
#define LST_OUT(slot)
#define PBAR(n) do { BAR(); if (P3EXIT == (n)) return; } while (0)
#define PBARC(n) do { BAR(); if (P3EXIT == (n)) { for (int a_ = 0; a_ < 5; ++a_) asm volatile("" :: "v"(acc[a_])); return; } } while (0)
template <bool LATE>
__device__ __forceinline__ void phase3(const Params& p, LAS char* lds, int tid, int lane, int w, int t0, int ct, u64 tag) {
    if (w == 9) {
        const int rb0 = t0 >> 7, rb1 = (t0 + P3_R - 1 < S ? t0 + P3_R - 1 : S - 1) >> 7;
        const char* fl = p.ws + WS_FO + (((lane & 8) ? rb1 : rb0) * 16 + (lane & 7)) * 128;
        LST_DECL; LST(0);
        wait_flags(fl, lane < 16, tag, p.ws);
        if (lane == 0) __hip_atomic_store((LAS unsigned*)(lds + L_RDY), 1u, __ATOMIC_RELAXED, __HIP_MEMORY_SCOPE_WORKGROUP);
        LST(1);
        wait_flags(fl + 8 * 128, lane < 16, tag, p.ws);
        if (lane == 0) __hip_atomic_store((LAS unsigned*)(lds + L_RDY + 4), 1u, __ATOMIC_RELAXED, __HIP_MEMORY_SCOPE_WORKGROUP);
        LST(2);
        LST_OUT(0);
        asm volatile("s_waitcnt lgkmcnt(0) vmcnt(0)" ::: "memory");
        __builtin_amdgcn_endpgm();
    } else if (w == 8) {
        const unsigned lbase = (unsigned)(size_t)lds;
        const __amdgpu_buffer_rsrc_t rwa = __builtin_amdgcn_make_buffer_rsrc((void*)p.woA, 0, D * D * 2, 0x00020000);
        const __amdgpu_buffer_rsrc_t rwb = __builtin_amdgcn_make_buffer_rsrc((void*)p.woB, 0, D * D * 2, 0x00020000);
        const int wrow = lane >> 3;
        const unsigned vwo0 = wrow * 1152 + ct * 128 + (((lane & 7) ^ (((wrow >> 1) & 1) << 1)) << 4), vwo1 = vwo0 ^ 64;
        auto issue_w = [&](int q) {
            const unsigned lb = lbase + P3_W0 + (q & 1) * P3_WQ, so = (q & 1) * 288 * 1152;
#pragma unroll 2
            for (int u = 0; u < 36; ++u) glds16(lb + u * 1024, (u & 1) ? vwo1 : vwo0, q < 2 ? rwa : rwb, so + u * 8 * 1152);
        };
        __builtin_amdgcn_s_setprio(3);
        issue_w(0); issue_w(1); WAITV(36); PBAR(1);
        WAITV(0); PBAR(2);
        issue_w(2); WAITV(0); PBAR(3);
        issue_w(3); WAITV(0); PBAR(4);
    } else if (w >= 4) {
        const __amdgpu_buffer_rsrc_t ro = __builtin_amdgcn_make_buffer_rsrc((void*)(p.ws + WS_O), 0, 16 * 2048 * 144, 0x00020000);
        const int t4 = tid - 256;
        unsigned oo[11], ol[11];
#pragma unroll
        for (int j = 0; j < 11; ++j) { const int c = t4 + 256 * j, sl = (c * 6133) >> 22, wi = c - P3_GC * sl;
            oo[j] = c < 4 * P3_GC ? (sl * 2048 + t0) * 144 + wi * 16 : 0xC0000000u; ol[j] = c < 4 * P3_GC ? sl * P3_GB + wi * 16 : P3_END + 128 + (lane & 7) * 16; }
        u32x4 x[11], y[11];
        auto load_o = [&](u32x4 (&r)[11], int q) {
#pragma unroll
            for (int j = 0; j < 11; ++j) r[j] = __builtin_amdgcn_raw_buffer_load_b128(ro, oo[j], q * (4 * 2048 * 144), 16);
            __builtin_amdgcn_sched_barrier(0);
        };
        auto write_ob = [&](const u32x4 (&r)[11], int buf) {
#pragma unroll
            for (int j = 0; j < 10; ++j) *(LAS u32x4*)(lds + buf * P3_OQ + ol[j]) = r[j];
            *(LAS u32x4*)(lds + (t4 + 2560 < 4 * P3_GC ? buf * P3_OQ : 0) + ol[10]) = r[10];
            __builtin_amdgcn_sched_barrier(0);
        };
        LST_DECL; LST(0);
        lds_wait(lds, L_RDY);
        load_o(x, 0); load_o(y, 1);
        LST(1);
        write_ob(x, 0);
        LST(2);
        PBAR(1);
        if (LATE) { lds_wait(lds, L_RDY + 4); load_o(x, 2); }
        LST(3);
        write_ob(y, 1);
        if (LATE) load_o(y, 3);
        LST(4);
        PBAR(2);
        if (!LATE) { lds_wait(lds, L_RDY + 4); load_o(x, 2); load_o(y, 3); }
        LST(5);
        write_ob(x, 0);
        LST(6);
        PBAR(3);
        write_ob(y, 1);
        LST(7);
        PBAR(4);
        if (w == 4) LST_OUT(2);
    } else {
        const int fg = lane >> 4, l15 = lane & 15;
        const int fl2 = ((l15 >> 3) & 1) | ((fg & 1) << 1);
        const unsigned wl0 = P3_W0 + (72 * fg + (l15 >> 2)) * 128 + ((w ^ fl2) << 5) + 8 * (lane & 3);
        const unsigned ofb = fg * P3_GB + l15 * 144;
        f32x4 acc[5];
#pragma unroll
        for (int a = 0; a < 5; ++a) acc[a] = f32x4{0, 0, 0, 0};
        struct F3 { bf16x8 w, o[5]; };
        auto compute = [&](int qodd) {
            const unsigned we = wl0 + qodd * P3_WQ, wo_ = we ^ 64, ob = ofb + qodd * P3_OQ;
            auto rd = [&](F3& f, int j) { const unsigned wa = ((j & 1) ? wo_ : we) + j * 1024;
                f.w = cat(trread(lds + wa), trread(lds + wa + 512));
#pragma unroll
                for (int a = 0; a < 5; ++a) f.o[a] = *(const LAS bf16x8*)(lds + ob + j * 16 + a * 16 * 144); };
            auto mm = [&](const F3& f) {
#pragma unroll
                for (int a = 0; a < 5; ++a) acc[a] = mfma16(f.w, f.o[a], acc[a]); };
            F3 f0, f1, f2;
            rd(f0, 0); rd(f1, 1);
            __builtin_amdgcn_sched_barrier(0);
#define P3STEP(FA, FC, j) do { if ((j) + 2 < 9) rd(FC, (j) + 2); mm(FA); \
            __builtin_amdgcn_sched_group_barrier(0x100, 2, 0); __builtin_amdgcn_sched_group_barrier(0x008, 1, 0); __builtin_amdgcn_sched_group_barrier(0x100, 2, 0); __builtin_amdgcn_sched_group_barrier(0x008, 1, 0); \
            __builtin_amdgcn_sched_group_barrier(0x100, 1, 0); __builtin_amdgcn_sched_group_barrier(0x008, 1, 0); __builtin_amdgcn_sched_group_barrier(0x100, 2, 0); __builtin_amdgcn_sched_group_barrier(0x008, 2, 0); \
            __builtin_amdgcn_sched_barrier(0); } while (0)
            P3STEP(f0, f2, 0); P3STEP(f1, f0, 1); P3STEP(f2, f1, 2); P3STEP(f0, f2, 3); P3STEP(f1, f0, 4); P3STEP(f2, f1, 5); P3STEP(f0, f2, 6); P3STEP(f1, f0, 7); P3STEP(f2, f1, 8);
#undef P3STEP
        };
        LST_DECL;
#pragma unroll 1
        for (int hf = 0; hf < 2; ++hf) {
            PBARC(1 + 2 * hf);
            compute(0);
            PBARC(2 + 2 * hf);
            compute(1);
        }
        if (P3EXIT == 5) { for (int a_ = 0; a_ < 5; ++a_) asm volatile("" :: "v"(acc[a_])); return; }
        const __amdgpu_buffer_rsrc_t rout = __builtin_amdgcn_make_buffer_rsrc(p.out, 0, S * D * 2, 0x00020000);
#pragma unroll
        for (int a = 0; a < 5; ++a) { const int tr = 16 * a + l15, trow = t0 + tr;
            __builtin_amdgcn_raw_buffer_store_b64(u32x2{pack2(acc[a][0], acc[a][1]), pack2(acc[a][2], acc[a][3])}, rout,
                                                  (tr < P3_R && trow < S) ? trow * 1152 + ct * 128 + w * 32 + fg * 8 : 0xFFFFFF00u, 0, OUT_AUX); }
        LST(7);
        if (w == 0) LST_OUT(1);
    }
}

__global__ __launch_bounds__(640, 3) void k_fused(Params p) {
    extern __shared__ __attribute__((aligned(16))) char smem[];
    LAS char* lds = (LAS char*)smem;
    const int tid = threadIdx.x, lane = tid & 63, w = __builtin_amdgcn_readfirstlane(tid >> 6);
    const int bid = blockIdx.x, g = bid & 7, idx = bid >> 3, br = idx & 1, rb = idx >> 1, r0 = rb * 128;
    const int s0 = (rb * 5 + g * 2 + br * 4) % 9;
    STAMP_INIT();
    STAMP(0);
    u64 tag;
    const __amdgpu_buffer_rsrc_t rws = __builtin_amdgcn_make_buffer_rsrc((void*)p.ws, 0, 16 << 20, 0x00020000);
    if (w >= 8) {
        if (w == 8 && lane == 0) *(LAS unsigned*)(lds + L_G + 512) = 0u;
        tag = (w == 8) ? load_loop<0>(p, lds, lane, g, br, r0, s0) : load_loop<1>(p, lds, lane, g, br, r0, s0);
        if (EXIT_AT == 1) return;
        BAR();
        if (w == 8 && lane < 49) *(LAS u32x4*)(lds + L_V + 72 * VROW + lane * 16) = u32x4{0x3F803F80u, 0x3F803F80u, 0x3F803F80u, 0x3F803F80u};
        if (!br) {
            const int r31 = lane & 31, h = lane >> 5, hh = (r31 >> 2) & 1, i = (r31 & 3) + 4 * (r31 >> 3); const bool valid = i <= 8 && r31 < 24; const int f = valid ? 9 * hh + i : 0;
            const unsigned m = valid ? 0xFFFFFFFFu : 0u;
            unsigned v[2][10];
#pragma unroll
            for (int e = 0; e < 2; ++e) { const LAS unsigned short* wp = (const LAS unsigned short*)(lds + L_WPHI) + (2 * (w - 8) + e) * 324 + f;
#pragma unroll
                for (int j = 0; j < 8; ++j) v[e][j] = wp[(8 * h + j + 2 * e) * 18];
                v[e][8] = wp[(e ? 0 : 16) * 18]; v[e][9] = wp[(e ? 1 : 17) * 18]; }
#pragma unroll
            for (int e = 0; e < 2; ++e) { LAS char* dst = lds + L_K + (2 * (w - 8) + e) * 18432 + 10240;
                *(LAS u32x4*)(dst + lane * 16) = u32x4{(v[e][0] | v[e][1] << 16) & m, (v[e][2] | v[e][3] << 16) & m, (v[e][4] | v[e][5] << 16) & m, (v[e][6] | v[e][7] << 16) & m};
                *(LAS u32x2*)(dst + 1024 + lane * 8) = u32x2{h ? 0u : (v[e][8] | v[e][9] << 16) & m, 0u}; }
        }
    } else {
        tag = *(const __attribute__((address_space(4))) u64*)(p.ws + WS_EPOCH) + 1;
        asm volatile("" :: "s"(tag));
        f32x4 acc[4][4];
#pragma unroll
        for (int t = 0; t < 4; ++t)
#pragma unroll
            for (int n = 0; n < 4; ++n) acc[t][n] = f32x4{0, 0, 0, 0};
        unsigned a0[4], a1[4];
#define RUN(V) do { image_addr<V>(w, lane, a0, a1); gemm_loop<V>(lds, w, lane, s0, acc); STAMP(1); if (EXIT_AT == 1) { for (int t_ = 0; t_ < 4; ++t_) for (int n_ = 0; n_ < 4; ++n_) asm volatile("" :: "v"(acc[t_][n_])); return; } BAR(); write_images<V>(lds, w, lane, br, a0, a1, acc); } while (0)
        const int v = ((w >> 1) & 1) * 2 + (w >> 2);
        if (v == 0) RUN(0); else if (v == 1) RUN(1); else if (v == 2) RUN(2); else RUN(3);
#undef RUN
    }
    BAR();
    STAMP(2);
    if (EXIT_AT == 2) return;
    if (EXIT_AT == 8 && !br) return;
    if (EXIT_AT >= 9 && br) return;
    const int head = w & 3, h = lane >> 5, r31 = lane & 31;
    LAS char* ob = lds + L_Q;
    if (br) {
        if (rb < 15 && (w < 4 || w >= 8)) {
            const unsigned kg = (unsigned)WS_KG + (rb * 8 + g) * KG_BYTES; const int t6 = (w < 4 ? w : w - 4) * 64 + lane;
            u32x4 pv[7];
#pragma unroll
            for (int j = 0; j < 4; ++j) pv[j] = *(const LAS u32x4*)(lds + L_K + j * 18432 + 12288 + t6 * 16);
#pragma unroll
            for (int j = 0; j < 3; ++j) { const int cc = t6 + 384 * j; pv[4 + j] = *(const LAS u32x4*)(lds + L_V + (cc >> 4) * VROW + 512 + (cc & 15) * 16); }
#pragma unroll
            for (int j = 0; j < 7; ++j) __builtin_amdgcn_raw_buffer_store_b128(pv[j], rws, kg + (t6 + 384 * j) * 16, 0, 16);
        }
        if (w >= 8) {
            const int t2 = tid - 512;
            KV_ARRIVE();
            if (EXIT_AT == 3) return;
            if (w == 8) STAMPW(11, p.ws);
            if (rb > 0) wait_flags(p.ws + WS_FKV + ((rb - 1 - (lane & 1)) * 8 + g) * 128, lane < 2 && rb - 1 - lane >= 0, tag, p.ws);
            if (w == 8) STAMPW(12, p.ws);
#pragma unroll
            for (int b = 0; b < 2; ++b) {
                u32x4 v[21];
                if (rb > b) {
                    const unsigned src = (unsigned)WS_KG + ((rb - 1 - b) * 8 + g) * KG_BYTES + t2 * 16;
#pragma unroll
                    for (int j = 0; j < 21; ++j) v[j] = __builtin_amdgcn_raw_buffer_load_b128(rws, src + j * 2048, 0, 16);
                    __builtin_amdgcn_sched_barrier(0);
#pragma unroll
                    for (int j = 0; j < 12; ++j) { const int c = t2 + 128 * j, hd = j / 3, wi = c - 384 * hd;
                        *(LAS u32x4*)(lds + L_K + hd * 18432 + (1 - b) * 6144 + wi * 16) = v[j]; }
#pragma unroll
                    for (int j = 0; j < 9; ++j) { const int c = t2 + 128 * j;
                        *(LAS u32x4*)(lds + L_V + (c >> 4) * VROW + (1 - b) * 256 + (c & 15) * 16) = v[12 + j]; }
                }
                if (w == 8) STAMPW(13 + b, p.ws);
                BAR();
                if (EXIT_AT == 4 + b) return;
            }
        } else {
            if (w < 4) attn_wave<0>(p, lds, w, lane, rb, g, tag); else attn_wave<1>(p, lds, w, lane, rb, g, tag);
        }
    } else {
        const int fg = lane >> 4, l15 = lane & 15, th = (w >> 2) & 1;
        LAS char* kvb = lds + L_V;
        LAS char* pt = lds + L_K + head * 18432;
        unsigned pq[2][9];
        f32x4 kv[3][2];
        bf16x8 wf0; bf16x4 wf1;
        if (w < 8) {
            wf0 = *(const LAS bf16x8*)(pt + 10240 + lane * 16); wf1 = *(const LAS bf16x4*)(pt + 11264 + lane * 8);
            unsigned pk[2][9];
            features2(lds + L_K + head * 18432 + (256 + 64 * th + r31) * 48, 32 * 48, lane, wf0, wf1, pk);
            const LAS char* vb0p = lds + L_V + (head * 18 + l15) * VROW + (256 + 64 * th + 8 * fg) * 2;
            const LAS char* vb1p = lds + L_V + (l15 < 2 ? head * 18 + 16 + l15 : 72) * VROW + (256 + 64 * th + 8 * fg) * 2;
            bf16x8 vb0[2], vb1[2];
#pragma unroll
            for (int tb = 0; tb < 2; ++tb) { vb0[tb] = *(const LAS bf16x8*)(vb0p + tb * 64); vb1[tb] = *(const LAS bf16x8*)(vb1p + tb * 64); }
#pragma unroll
            for (int tb = 0; tb < 2; ++tb) {
                LAS char* wr = pt + (2 * th + tb) * 2560 + r31 * 80 + 40 * h;
                *(LAS u32x2*)(wr) = u32x2{pk[tb][0], pk[tb][1]}; *(LAS u32x2*)(wr + 8) = u32x2{pk[tb][2], pk[tb][3]}; *(LAS u32x2*)(wr + 16) = u32x2{pk[tb][4], pk[tb][5]};
                *(LAS u32x2*)(wr + 24) = u32x2{pk[tb][6], pk[tb][7]}; *(LAS unsigned*)(wr + 32) = pk[tb][8];
            }
            const LAS char* tr = pt + 2 * th * 2560 + (16 * (fg >> 1) + 4 * (fg & 1) + (l15 >> 2)) * 80 + 8 * (lane & 3);
            bf16x8 af[2][3];
#pragma unroll
            for (int tb = 0; tb < 2; ++tb)
#pragma unroll
                for (int ft = 0; ft < 3; ++ft) af[tb][ft] = cat(trread(tr + tb * 2560 + ft * 32), trread(tr + tb * 2560 + ft * 32 + 8 * 80));
#pragma unroll
            for (int ft = 0; ft < 3; ++ft) {
                kv[ft][0] = mfma16(af[1][ft], vb0[1], mfma16(af[0][ft], vb0[0], f32x4{0, 0, 0, 0}));
                kv[ft][1] = mfma16(af[1][ft], vb1[1], mfma16(af[0][ft], vb1[0], f32x4{0, 0, 0, 0}));
            }
            if (th == 1) {
#pragma unroll
                for (int ft = 0; ft < 3; ++ft) { *(LAS f32x4*)(pt + 5120 + (2 * ft) * 1024 + lane * 16) = kv[ft][0]; *(LAS f32x4*)(pt + 5120 + (2 * ft + 1) * 1024 + lane * 16) = kv[ft][1]; }
            }
        }
        BAR();
        if (EXIT_AT == 10) { asm volatile("" :: "v"(kv[0][0]), "v"(kv[1][0]), "v"(kv[2][0]), "v"(kv[0][1]), "v"(kv[1][1]), "v"(kv[2][1])); return; }
        if (w < 4) {
            f32x4 u[3][2];
#pragma unroll
            for (int ft = 0; ft < 3; ++ft) { u[ft][0] = *(const LAS f32x4*)(pt + 5120 + (2 * ft) * 1024 + lane * 16); u[ft][1] = *(const LAS f32x4*)(pt + 5120 + (2 * ft + 1) * 1024 + lane * 16); }
            LAS char* kr = pt;
#pragma unroll
            for (int ft = 0; ft < 3; ++ft)
#pragma unroll
                for (int dt = 0; dt < 2; ++dt) { const int drow = 16 * dt + l15, f = 16 * ft + 4 * fg; const f32x4 t = kv[ft][dt] + u[ft][dt];
                    *(LAS u32x2*)(kr + ((drow < 19 && f < 40) ? drow * 80 + f * 2 : 2048 + lane * 8)) = u32x2{pack2(t[0], t[1]), pack2(t[2], t[3])}; }
            const unsigned kvp = (unsigned)WS_KVP + (g * 16 + rb) * KVP_BYTES + head * 1520;
#pragma unroll
            for (int j = 0; j < 2; ++j) { const int c = lane + 64 * j;
                __builtin_amdgcn_raw_buffer_store_b128(*(const LAS u32x4*)(kr + c * 16), rws, c < 95 ? kvp + c * 16 : 0xFFFFFF00u, 0, 16); }
        }
        if (w < 8) features2(lds + L_Q + head * 6144 + (64 * th + r31) * 48, 32 * 48, lane, wf0, wf1, pq);
        if (w < 4) {
            WAITV(0);
            unsigned prev = 0;
            if (lane == 0) prev = __hip_atomic_fetch_add((LAS unsigned*)(lds + L_G + 512), 1u, __ATOMIC_RELAXED, __HIP_MEMORY_SCOPE_WORKGROUP);
            if (lane == 0 && prev == 3) set_flag(p.ws + WS_FKP + (rb * 8 + g) * 128, tag);
        }
        STAMP(3);
        if (w == 8) wait_flags(p.ws + WS_FKP + ((lane & 15) * 8 + g) * 128, lane < 16, tag, p.ws);
        BAR();
        STAMP(4);
        if (EXIT_AT == 11) { for (int a_ = 0; a_ < 2; ++a_) for (int b_ = 0; b_ < 9; ++b_) asm volatile("" :: "v"(pq[a_][b_])); return; }
        if (tid < KVP_CHUNKS) {
            u32x4 v[16];
#pragma unroll
            for (int i = 0; i < 16; ++i) v[i] = __builtin_amdgcn_raw_buffer_load_b128(rws, (unsigned)WS_KVP + (g * 16 + i) * KVP_BYTES + tid * 16, 0, 16);
            float sum[8];
#pragma unroll
            for (int e = 0; e < 8; ++e) sum[e] = 0.f;
#pragma unroll
            for (int i = 0; i < 16; ++i)
#pragma unroll
                for (int e = 0; e < 4; ++e) {
                    sum[2 * e] += __uint_as_float(v[i][e] << 16); sum[2 * e + 1] += __uint_as_float(v[i][e] & 0xffff0000u);
                }
            const int row = (tid * 13108) >> 16, f = (tid - 5 * row) * 8;
            LAS char* dst = kvb + (row / 5) * VROW + (row % 5) * 96;
            *(LAS u32x2*)(dst + (f >= 20 ? 8 + 2 * f : 2 * f)) = u32x2{pack2(sum[0], sum[1]), pack2(sum[2], sum[3])};
            *(LAS u32x2*)(dst + (f + 4 >= 20 ? 16 + 2 * f : 8 + 2 * f)) = u32x2{pack2(sum[4], sum[5]), pack2(sum[6], sum[7])};
        }
        BAR();
        STAMP(5);
        if (EXIT_AT == 12) { for (int a_ = 0; a_ < 2; ++a_) for (int b_ = 0; b_ < 9; ++b_) asm volatile("" :: "v"(pq[a_][b_])); return; }
        if (w < 8) {
            const int row = head * 19 + (r31 < 19 ? r31 : 18);
            const LAS char* ap = kvb + (row / 5) * VROW + (row % 5) * 96 + 48 * h;
            const bf16x8 a0 = *(const LAS bf16x8*)(ap), a1 = *(const LAS bf16x8*)(ap + 16);
            const bf16x4 a2 = __builtin_bit_cast(bf16x4, u32x2{*(const LAS unsigned*)(ap + 32), 0u});
            f32x16 o[2]; float gt[2];
#pragma unroll
            for (int tb = 0; tb < 2; ++tb) {
                gt[tb] = gate_at(lds, 64 * th + 32 * tb + r31);
                o[tb] = mfma32(a0, __builtin_bit_cast(bf16x8, u32x4{pq[tb][0], pq[tb][1], pq[tb][2], pq[tb][3]}), f32x16{});
                o[tb] = mfma32(a1, __builtin_bit_cast(bf16x8, u32x4{pq[tb][4], pq[tb][5], pq[tb][6], pq[tb][7]}), o[tb]);
                o[tb] = mfma32k8(a2, __builtin_bit_cast(bf16x4, u32x2{pq[tb][8], 0u}), o[tb]);
            }
#pragma unroll
            for (int tb = 0; tb < 2; ++tb) write_o(ob, lane, head, 64 * th + 32 * tb, o[tb], gt[tb] * __builtin_amdgcn_rcpf(swap_lo(o[tb][10]) + 1e-6f));
        }
    }
    BAR();
    STAMP(6);
    if (EXIT_AT == 6 || EXIT_AT == 8 || EXIT_AT == 9) return;
    { const unsigned od = (unsigned)WS_O + ((br * 8 + g) * 2048 + r0) * 144;
#pragma unroll
      for (int j = 0; j < 2; ++j) { const int c = tid + 640 * j;
          if (c < 1152) __builtin_amdgcn_raw_buffer_store_b128(*(const LAS u32x4*)(ob + c * 16), rws, od + c * 16, 0, 16); } }
    if (tid == 576) *(LAS u32x2*)(lds + L_RDY) = u32x2{0u, 0u};
    WAITV(0);
    BAR();
    if (tid == 0) set_flag(p.ws + WS_FO + (rb * 16 + br * 8 + g) * 128, tag);
    STAMP(7);
    if (EXIT_AT == 7) return;
    if (bid == 255 && w == 8) {
        for (unsigned spins = 0;;) {
            bool ok = true;
#pragma unroll
            for (int j = 0; j < 4; ++j) ok &= __hip_atomic_load((const GAS u64*)(p.ws + (j < 2 ? WS_FKV : WS_FKP - 16384) + (lane + 64 * j) * 128), __ATOMIC_RELAXED, __HIP_MEMORY_SCOPE_AGENT) == tag;
            if (__all(ok)) { if (lane == 0) set_flag(p.ws + WS_EPOCH, tag); break; }
            if (++spins > SPIN_CAP) break;
            __builtin_amdgcn_s_sleep(1);
        }
    }
    STAMP_DUMP();
    { const int xcd = bid & 7, ii = bid >> 3, t = xcd * 31 + ii;
      if (ii < 31 && t < 243) { if (br) phase3<true>(p, lds, tid, lane, w, (t / 9) * P3_R, t % 9, tag); else phase3<false>(p, lds, tid, lane, w, (t / 9) * P3_R, t % 9, tag); } }
}

extern "C" void kernel_launch(void* const* d_in, const int* in_sizes, int n_in, void* d_out, int out_size, void* d_ws, size_t ws_size, hipStream_t stream) {
    Params p;
    p.x = d_in[0]; p.wqkvA = d_in[1]; p.wphi = d_in[2]; p.woA = d_in[3]; p.wqkvB = d_in[5]; p.woB = d_in[6]; p.gw = d_in[7];
    p.out = d_out; p.ws = (char*)d_ws;
    hipFuncSetAttribute((const void*)k_fused, hipFuncAttributeMaxDynamicSharedMemorySize, LDS_BYTES);
    k_fused<<<256, 640, LDS_BYTES, stream>>>(p);
}
```

```cpp
#include <hip/hip_runtime.h>
#include <stdint.h>
#include <type_traits>
#define USE_DOT2 0
#define FORCE_FB 0
#ifndef EXIT_AT
#define EXIT_AT 0
#endif
#define NOGEMM 0
#define OUT_AUX 0
#define GPRIO 1
#define APRIO 1
#define XMAP 0
#define P3LATE_ALL 1
#define W1LATE 0
#define KPRIO 1
#define O_AUX 16
#define ACQ 0
#define O_ST_AUX 16
#define ROT 1

#define LAS __attribute__((address_space(3)))
#define GAS __attribute__((address_space(1)))
typedef short bf16x8 __attribute__((ext_vector_type(8)));
typedef short bf16x4 __attribute__((ext_vector_type(4)));
typedef float f32x2 __attribute__((ext_vector_type(2)));
typedef float f32x4 __attribute__((ext_vector_type(4)));
typedef float f32x16 __attribute__((ext_vector_type(16)));
typedef unsigned u32x2 __attribute__((ext_vector_type(2)));
typedef unsigned u32x4 __attribute__((ext_vector_type(4)));
typedef __bf16 bf2_t __attribute__((ext_vector_type(2)));
typedef unsigned long long u64;

constexpr int S = 2048, D = 576;
constexpr float LOG2E = 1.4426950408889634f;
constexpr float QSCALE_B = 0.23570226039551584f * LOG2E;
constexpr float NEG = -1.0e30f;

constexpr size_t WS_EPOCH = 0, WS_FKV = 4096, WS_FKP = 20480, WS_FO = 36864, WS_TMO = 69632;
constexpr size_t WS_KG = 131072;
constexpr int KG_BYTES = 43008, KG_K = 24576;
constexpr size_t WS_KVP = 6291456;
constexpr int KVP_BYTES = 6144, KVP_CHUNKS = 380;
constexpr size_t WS_O = 8388608;
constexpr size_t WS_STAMP = 16777216;

constexpr int XS = 16384, WSB = 27648, BUFS = XS + WSB;
constexpr int L_K = 0;
constexpr int L_V = 73728, VROW = 784;
constexpr int L_Q = 130960;
constexpr int L_G = 155648;
constexpr int L_GW = 158720;
constexpr int L_WPHI = 160768;
constexpr int LDS_BYTES = 163840;
constexpr unsigned SPIN_CAP = 1u << 18;

struct Params {
    const void* x; const void* wqkvA; const void* wphi; const void* woA; const void* wqkvB; const void* woB; const void* gw;
    void* out; char* ws;
};

__device__ __forceinline__ unsigned pack2(float a, float b) { f32x2 v = {a, b}; bf2_t r = __builtin_convertvector(v, bf2_t); return __builtin_bit_cast(unsigned, r); }
__device__ __forceinline__ void glds16(unsigned ldsaddr, unsigned voff, __amdgpu_buffer_rsrc_t rsrc, unsigned soff) {
    asm volatile("s_mov_b32 m0, %0\n\ts_nop 0\n\tbuffer_load_dwordx4 %1, %2, %3 offen lds" :: "s"(ldsaddr), "v"(voff), "s"(rsrc), "s"(soff) : "memory");
}
#define BAR() asm volatile("s_waitcnt lgkmcnt(0)\n\ts_barrier" ::: "memory")
#define WAITV(n) asm volatile("s_waitcnt vmcnt(" #n ")" ::: "memory")
__device__ __forceinline__ bf16x4 trread(const LAS char* p) { typedef short v4s __attribute__((ext_vector_type(4))); return __builtin_amdgcn_ds_read_tr16_b64_v4i16((LAS v4s*)p); }
__device__ __forceinline__ bf16x8 cat(bf16x4 a, bf16x4 b) { return __builtin_shufflevector(a, b, 0, 1, 2, 3, 4, 5, 6, 7); }
__device__ __forceinline__ f32x4 mfma16(bf16x8 a, bf16x8 b, f32x4 c) { return __builtin_amdgcn_mfma_f32_16x16x32_bf16(a, b, c, 0, 0, 0); }
__device__ __forceinline__ f32x16 mfma32(bf16x8 a, bf16x8 b, f32x16 c) { return __builtin_amdgcn_mfma_f32_32x32x16_bf16(a, b, c, 0, 0, 0); }
__device__ __forceinline__ f32x16 mfma32k8(bf16x4 a, bf16x4 b, f32x16 c) { return __builtin_amdgcn_mfma_f32_32x32x8bf16_1k(a, b, c, 0, 0, 0); }
__device__ __forceinline__ float swap_lo(float v) {
    auto r = __builtin_amdgcn_permlane32_swap(__float_as_uint(v), __float_as_uint(v), false, false); return __uint_as_float(r[0]); }
__device__ __forceinline__ float swap_max(float v) { auto r = __builtin_amdgcn_permlane32_swap(__float_as_uint(v), __float_as_uint(v), false, false); return fmaxf(__uint_as_float(r[0]), __uint_as_float(r[1])); }
__device__ __forceinline__ float swap_min(float v) { auto r = __builtin_amdgcn_permlane32_swap(__float_as_uint(v), __float_as_uint(v), false, false); return fminf(__uint_as_float(r[0]), __uint_as_float(r[1])); }
__device__ __forceinline__ float swap_sum(float v) { auto r = __builtin_amdgcn_permlane32_swap(__float_as_uint(v), __float_as_uint(v), false, false); return __uint_as_float(r[0]) + __uint_as_float(r[1]); }

__device__ __forceinline__ bool wait_flags(const char* p, bool active, u64 tag, char* ws) {
    for (unsigned spins = 0;;) {
        u64 v = active ? __hip_atomic_load((const GAS u64*)p, __ATOMIC_RELAXED, __HIP_MEMORY_SCOPE_AGENT) : tag;
        if (__all(v == tag)) return true;
        __builtin_amdgcn_s_sleep(1);
        if (++spins > SPIN_CAP) { __hip_atomic_store((GAS unsigned*)(ws + WS_TMO), 1u, __ATOMIC_RELAXED, __HIP_MEMORY_SCOPE_AGENT); return false; }
    }
}
__device__ __forceinline__ void set_flag(char* p, u64 tag) { __hip_atomic_store((GAS u64*)p, tag, __ATOMIC_RELAXED, __HIP_MEMORY_SCOPE_AGENT); }

#define SPAN_END() do {} while (0)
#define STAMP(i) do {} while (0)
#define STAMP9(i) do {} while (0)
#define STAMPW(i, ws_) do {} while (0)
#define STAMP_DUMP() do {} while (0)
#define STAMP_INIT() do {} while (0)

template <int V> struct WT { static constexpr int T0 = V == 0 ? 0 : V == 1 ? 4 : V == 2 ? 7 : 11, NC = (V & 1) ? 3 : 4; };
constexpr int WSEC = 64 * 144;
template <int V> struct Frags { bf16x8 x[4], w[WT<V>::NC]; };
template <int V>
__device__ __forceinline__ void read_frags(Frags<V>& f, const LAS char* xp, const LAS char* wp, const LAS char* wp4, const LAS char* gp, bool gsel) {
#pragma unroll
    for (int t = 0; t < 4; ++t) f.x[t] = *(const LAS bf16x8*)(xp + t * 2048);
#pragma unroll
    for (int n = 0; n < WT<V>::NC; ++n) {
        constexpr int T0 = WT<V>::T0; const int T = T0 + n, sec = (16 * T) / 72, off = sec * WSEC + (16 * T - 72 * sec) * 2;
        if (T == 4) f.w[n] = cat(trread(wp4), trread(wp4 + 144));
        else f.w[n] = cat(trread(wp + off), trread(wp + off + 144));
    }
    if (V == 3) { const bf16x8 gf = *(const LAS bf16x8*)(gp); const u32x4 a = __builtin_bit_cast(u32x4, f.w[2]), b = __builtin_bit_cast(u32x4, gf);
        f.w[2] = __builtin_bit_cast(bf16x8, u32x4{gsel ? b[0] : a[0], gsel ? b[1] : a[1], gsel ? b[2] : a[2], gsel ? b[3] : a[3]}); }
}
template <int V>
__device__ __forceinline__ void mma_frags(const Frags<V>& f, f32x4 (&acc)[4][4]) {
#pragma unroll
    for (int n = 0; n < WT<V>::NC; ++n)
#pragma unroll
        for (int t = 0; t < 4; ++t) acc[t][n] = (WT<V>::T0 + n <= 8) ? mfma16(f.w[n], f.x[t], acc[t][n]) : mfma16(f.x[t], f.w[n], acc[t][n]);
}
template <int V>
__device__ __forceinline__ void interleave() {
#define SG1 __builtin_amdgcn_sched_group_barrier(0x008, 1, 0); __builtin_amdgcn_sched_group_barrier(0x100, 1, 0);
    SG1 SG1 SG1 SG1 SG1 SG1 SG1 SG1 SG1 SG1
    if (V != 1) { SG1 }
    if (V == 0 || V == 2) { SG1 }
#undef SG1
}
template <int V>
__device__ __forceinline__ void gemm_loop(LAS char* lds, int w, int lane, int s0, f32x4 (&acc)[4][4]) {
    const int fg = lane >> 4, l15 = lane & 15, pp = lane & 3;
    const unsigned sw = (l15 >> 1) & 7;
    const unsigned xb0 = (64 * (w & 1) + l15) * 128 + ((fg ^ sw) << 4), xb1 = xb0 ^ 64;
    const unsigned wb = XS + (8 * fg + 2 * (l15 >> 2)) * 144 + 8 * pp;
    const unsigned wb4 = wb + (pp < 2 ? 128 : WSEC - 16);
    const LAS char* gp0 = lds + L_GW + 16 * fg;
    const LAS char* gp = gp0 + s0 * 128;
    int ks = s0;
    const bool gsel = l15 >= 8;
    unsigned cur = 0;
    Frags<V> f0, f1;
    BAR();
    if (EXIT_AT == 20) return;
    read_frags<V>(f0, lds + xb0, lds + wb, lds + wb4, gp, gsel);
#pragma unroll 1
    for (int s = 0; s < 9; ++s) {
        read_frags<V>(f1, lds + cur + xb1, lds + cur + wb + 32 * 144, lds + cur + wb4 + 32 * 144, gp + 64, gsel);
        mma_frags<V>(f0, acc);
        interleave<V>();
        __builtin_amdgcn_sched_barrier(0);
        cur = (cur == 2 * BUFS) ? 0 : cur + BUFS; ks = (ks == 8) ? 0 : ks + 1; gp = gp0 + ks * 128;
        if (s < 8) BAR();
        read_frags<V>(f0, lds + cur + xb0, lds + cur + wb, lds + cur + wb4, gp, gsel);
        mma_frags<V>(f1, acc);
        interleave<V>();
        __builtin_amdgcn_sched_barrier(0);
    }
}

template <int LW>
__device__ __forceinline__ u64 load_loop(const Params& p, LAS char* lds, int lane, int g, int br, int r0, int s0) {
    constexpr int NV = LW ? 4 : 5;
    const __amdgpu_buffer_rsrc_t rx = __builtin_amdgcn_make_buffer_rsrc((void*)p.x, 0, S * D * 2, 0x00020000);
    const __amdgpu_buffer_rsrc_t rw = __builtin_amdgcn_make_buffer_rsrc((void*)(br ? p.wqkvB : p.wqkvA), 0, D * 3 * D * 2, 0x00020000);
    const unsigned lbase = (unsigned)(size_t)lds;
    const unsigned vx = (r0 + 8 * LW + (lane >> 3)) * 1152 + (((lane & 7) ^ ((4 * LW + (lane >> 4)) & 7)) << 4);
    auto issue_x = [&](int s, unsigned buf) {
#pragma unroll
        for (int i = 0; i < 8; ++i) glds16(lbase + buf + LW * 1024 + i * 2048, vx, rx, s * 128 + i * (16 * 1152));
    };
    STAMP(8);
    const int s1 = s0 == 8 ? 0 : s0 + 1;
    issue_x(s0, 0);
    unsigned vw[NV];
#pragma unroll
    for (int j = 0; j < NV; ++j) {
        const int ci = 64 * (LW + 2 * j) + lane, rho = (ci * 7282) >> 16, jj = ci - 9 * rho;
        const int k = (rho & ~7) + ((rho & 1) << 2) + ((rho >> 1) & 3);
        vw[j] = k * 3456 + g * 144 + jj * 16;
    }
    auto issue_w = [&](int s, unsigned buf) {
#pragma unroll
        for (int sec = 0; sec < 3; ++sec)
#pragma unroll
            for (int j = 0; j < NV; ++j) glds16(lbase + buf + XS + sec * WSEC + (LW + 2 * j) * 1024, vw[j], rw, s * (64 * 3456) + sec * 1152);
    };
    issue_w(s0, 0);
    if (LW == 0) { const __amdgpu_buffer_rsrc_t rg = __builtin_amdgcn_make_buffer_rsrc((void*)p.gw, 0, D * 2, 0x00020000);
        glds16(lbase + L_GW, lane * 16, rg, 0); glds16(lbase + L_GW + 1024, lane * 16, rg, 1024); }
    else if (!br) { const __amdgpu_buffer_rsrc_t rp = __builtin_amdgcn_make_buffer_rsrc((void*)p.wphi, 0, 32 * 324 * 2, 0x00020000);
#pragma unroll
        for (int i = 0; i < 3; ++i) glds16(lbase + L_WPHI + i * 1024, lane * 16, rp, g * 2592 + i * 1024); }
    issue_x(s1, BUFS); issue_w(s1, BUFS);
    STAMP(9);
    const u64 tag = *(const __attribute__((address_space(4))) u64*)(p.ws + WS_EPOCH) + 1;
    asm volatile("" :: "s"(tag));
    unsigned nxt2 = 2 * BUFS; int k2 = s1 == 8 ? 0 : s1 + 1;
#pragma unroll 1
    for (int s = 0; s < 9; ++s) {
        if (s < 8) { if (LW) WAITV(20); else WAITV(23); } else WAITV(0);
        if (s == 0) STAMP(10);
        STAMP9(s);
        BAR();
        if (EXIT_AT == 20) return tag;
        STAMP9(9 + s);
        if (s + 2 < 9) { issue_x(k2, nxt2); issue_w(k2, nxt2); }
        nxt2 = (nxt2 == 2 * BUFS) ? 0 : nxt2 + BUFS; k2 = k2 == 8 ? 0 : k2 + 1;
    }
    return tag;
}

template <int V>
__device__ __forceinline__ void image_addr(int w, int lane, unsigned (&a0)[4], unsigned (&a1)[4]) {
    const int fg = lane >> 4, l15 = lane & 15;
#pragma unroll
    for (int n = 0; n < 4; ++n) { a0[n] = 0; a1[n] = 0; }
#pragma unroll
    for (int n = 0; n < WT<V>::NC; ++n) {
        const int T = WT<V>::T0 + n;
        if (T <= 8) {
            const int G = 4 * T + fg, q4 = (G * 57) >> 9, m = G - 9 * q4, sec = q4 >> 1, hp = q4 & 1;
            const int hstride = sec ? 384 * 48 : 128 * 48;
            const int base = (sec ? L_K + 256 * 48 : L_Q) + 2 * hp * hstride + (64 * (w & 1) + l15) * 48;
            a0[n] = base + (m < 4 ? 8 * m : (m == 4 ? 32 : hstride + 8 * (m - 5)));
            a1[n] = (m == 4) ? base + hstride + 32 : a0[n] + 4;
            asm volatile("" : "+v"(a0[n])); if (T & 1) asm volatile("" : "+v"(a1[n]));
        }
    }
    if (V >= 2) { a0[3] = L_V + l15 * VROW + (256 + 64 * (w & 1) + 4 * (((fg & 1) << 1) | (fg >> 1))) * 2; asm volatile("" : "+v"(a0[3])); }
}
template <int V>
__device__ __forceinline__ void write_images(LAS char* lds, int w, int lane, int br, const unsigned (&a0)[4], const unsigned (&a1)[4], const f32x4 (&acc)[4][4]) {
    const int fg = lane >> 4, l15 = lane & 15;
    const float qs = br ? QSCALE_B : LOG2E, ks = br ? 1.0f : LOG2E;
#pragma unroll
    for (int n = 0; n < WT<V>::NC; ++n) {
        const int T = WT<V>::T0 + n;
        if (T <= 8) {
            const float sc = T < 4 ? qs : (T > 4 ? ks : (fg < 2 ? qs : ks));
#pragma unroll
            for (int tt = 0; tt < 4; ++tt) {
                const unsigned lo = pack2(acc[tt][n][0] * sc, acc[tt][n][1] * sc), hi = pack2(acc[tt][n][2] * sc, acc[tt][n][3] * sc);
                if (T & 1) { *(LAS unsigned*)(lds + a0[n] + tt * 768) = lo; *(LAS unsigned*)(lds + a1[n] + tt * 768) = hi; }
                else *(LAS u32x2*)(lds + a0[n] + tt * 768) = u32x2{lo, hi};
            }
        } else if (T < 13 || l15 < 8) {
#pragma unroll
            for (int tt = 0; tt < 4; ++tt)
                *(LAS u32x2*)(lds + a0[3] + (T - 9) * 16 * VROW + tt * 32) = u32x2{pack2(acc[tt][n][0], acc[tt][n][1]), pack2(acc[tt][n][2], acc[tt][n][3])};
        }
    }
    if (V == 3 && l15 == 8) {
#pragma unroll
        for (int tt = 0; tt < 4; ++tt) *(LAS f32x4*)(lds + L_G + (64 * (w & 1) + 16 * tt + 4 * fg) * 4) = acc[tt][2];
    }
}
__device__ __forceinline__ float gate_at(const LAS char* lds, int tok) {
    return __builtin_amdgcn_rcpf(1.0f + __builtin_amdgcn_exp2f(-LOG2E * *(const LAS float*)(lds + L_G + tok * 4)));
}

__device__ __forceinline__ void write_o(LAS char* ob, int lane, int head, int tok0, const f32x16& o, float scale) {
    const int h = lane >> 5, tok = tok0 + (lane & 31);
    LAS char* a = ob + tok * 144 + head * 36 + 8 * h;
    *(LAS unsigned*)(a) = pack2(o[0] * scale, o[1] * scale);
    *(LAS unsigned*)(a + 4) = pack2(o[2] * scale, o[3] * scale);
    *(LAS unsigned*)(a + 16) = pack2(o[4] * scale, o[5] * scale);
    *(LAS unsigned*)(a + 20) = pack2(o[6] * scale, o[7] * scale);
    if (h == 0) *(LAS unsigned*)(a + 32) = pack2(o[8] * scale, o[9] * scale);
}

enum { M_NONE, M_ZERO, M_DIAG, M_TAIL };
struct AttnState { f32x16 oA, oB, sA, sB, mD; bf16x8 qA0, qB0; bf16x4 qA1, qB1; const LAS char* kp; const LAS char* k1p; const LAS char* vp; };
__device__ __forceinline__ void exp_pack(const f32x16& s, bf16x8& p0, bf16x8& p1) {
    u32x4 a, b;
#pragma unroll
    for (int i = 0; i < 4; ++i) {
        a[i] = pack2(__builtin_amdgcn_exp2f(s[2 * i]), __builtin_amdgcn_exp2f(s[2 * i + 1]));
        b[i] = pack2(__builtin_amdgcn_exp2f(s[8 + 2 * i]), __builtin_amdgcn_exp2f(s[9 + 2 * i]));
    }
    p0 = __builtin_bit_cast(bf16x8, a); p1 = __builtin_bit_cast(bf16x8, b);
}
template <bool TAIL>
__device__ __forceinline__ f32x16 make_mask(float rbig) {
    f32x16 m;
#pragma unroll
    for (int i = 0; i < 16; ++i) { const float t = rbig - (float)((i & 3) + 8 * (i >> 2)) * 1.2676506002282294e30f; m[i] = __builtin_amdgcn_fmed3f(TAIL ? -t : t, 0.f, -3.0e38f); }
    return m;
}
__device__ __forceinline__ f32x16 attn_slow(const LAS char* kp, const LAS char* vp, bf16x8 q0, bf16x4 q1, int kb0, int kbd, bool tail, int lane, float rbig) {
    const int h = lane >> 5;
    float m = -3.0e38f;
    f32x16 o = {};
#pragma unroll 1
    for (int pass = 0; pass < 2; ++pass) {
#pragma unroll 1
        for (int kb = kb0; kb <= kbd; ++kb) {
            const bf16x8 k0 = *(const LAS bf16x8*)(kp + kb * 1536);
            const bf16x4 k1 = __builtin_bit_cast(bf16x4, u32x2{*(const LAS unsigned*)(kp + kb * 1536 + 32 - 16 * h), 0u});
            f32x16 c = {};
            if (kb == kbd) c = make_mask<false>(rbig); else if (tail && kb == kb0) c = make_mask<true>(rbig);
            f32x16 sc = mfma32k8(k1, q1, mfma32(k0, q0, c));
            if (pass == 0) {
#pragma unroll
                for (int i = 0; i < 16; ++i) m = fmaxf(m, sc[i]);
            } else {
#pragma unroll
                for (int i = 0; i < 16; ++i) sc[i] -= m;
                bf16x8 p0, p1; exp_pack(sc, p0, p1);
                const bf16x8 v0 = *(const LAS bf16x8*)(vp + kb * 64), v1 = *(const LAS bf16x8*)(vp + kb * 64 + 32);
                o = mfma32(v0, p0, o); o = mfma32(v1, p1, o);
            }
        }
        if (pass == 0) m = swap_max(m);
    }
    return o;
}
template <int KB, int NA, int NB>
__device__ __forceinline__ void attn_scores(const AttnState& st, f32x16& nA, f32x16& nB) {
    const bf16x8 k0 = *(const LAS bf16x8*)(st.kp + KB * 1536);
    const bf16x4 k1 = __builtin_bit_cast(bf16x4, u32x2{*(const LAS unsigned*)(st.k1p + KB * 1536), 0u});
    if (NA != M_NONE) nA = mfma32k8(k1, st.qA1, mfma32(k0, st.qA0, NA == M_ZERO ? f32x16{} : st.mD));
    if (NB != M_NONE) nB = mfma32k8(k1, st.qB1, mfma32(k0, st.qB0, NB == M_ZERO ? f32x16{} : st.mD));
}
template <int KB, int NA, int NB>
__device__ __forceinline__ void attn_first(AttnState& st) { attn_scores<KB, NA, NB>(st, st.sA, st.sB); __builtin_amdgcn_sched_barrier(0); }
template <int KB, bool A, bool B, int NA, int NB>
__device__ __forceinline__ void attn_step(AttnState& st) {
    const bf16x8 v0 = *(const LAS bf16x8*)(st.vp + KB * 64), v1 = *(const LAS bf16x8*)(st.vp + KB * 64 + 32);
    f32x16 nA, nB;
    if (NA != M_NONE || NB != M_NONE) attn_scores<(KB > 0 ? KB - 1 : 0), NA, NB>(st, nA, nB);
    bf16x8 p0, p1;
    if (A) { exp_pack(st.sA, p0, p1); st.oA = mfma32(v0, p0, st.oA); st.oA = mfma32(v1, p1, st.oA); }
    if (B) { exp_pack(st.sB, p0, p1); st.oB = mfma32(v0, p0, st.oB); st.oB = mfma32(v1, p1, st.oB); }
    if (NA != M_NONE) st.sA = nA;
    if (NB != M_NONE) st.sB = nB;
    __builtin_amdgcn_sched_group_barrier(0x100, 4, 0);
#define SGA __builtin_amdgcn_sched_group_barrier(0x400, 4, 0); __builtin_amdgcn_sched_group_barrier(0x002, 2, 0); __builtin_amdgcn_sched_group_barrier(0x008, 1, 0);
    SGA SGA SGA SGA SGA SGA SGA SGA
#undef SGA
    __builtin_amdgcn_sched_barrier(0);
}

__device__ __forceinline__ void halo_first(const Params& p, LAS char* lds, int tid, int rb, int g) {
    const __amdgpu_buffer_rsrc_t rws = __builtin_amdgcn_make_buffer_rsrc((void*)p.ws, 0, 16 << 20, 0x00020000);
    const unsigned src = (unsigned)WS_KG + ((rb >= 1 ? rb - 1 : 0) * 8 + g) * KG_BYTES;
    u32x4 v[5]; unsigned la[5];
#pragma unroll
    for (int j = 0; j < 5; ++j) { const int c = tid + 640 * j; const bool ok = (j < 4 || c < 2688) && rb >= 1;
        v[j] = __builtin_amdgcn_raw_buffer_load_b128(rws, ok ? src + c * 16 : 0xFFFFFF00u, 0, 16);
        int a;
        if (j < 2 || (j == 2 && c < 1536)) { const int hd = (c * 683) >> 18, wi = c - 384 * hd; a = L_K + hd * 18432 + 6144 + wi * 16; }
        else { const int cc = c - 1536; a = L_V + (cc >> 4) * VROW + 256 + (cc & 15) * 16; }
        la[j] = ok ? a : L_G + 1024 + (tid & 15) * 16; }
    __builtin_amdgcn_sched_barrier(0);
#pragma unroll
    for (int j = 0; j < 5; ++j) *(LAS u32x4*)(lds + la[j]) = v[j];
}
#define KV_ARRIVE() do { WAITV(0); unsigned prev = 0; \
            if (lane == 0) prev = __hip_atomic_fetch_add((LAS unsigned*)(lds + L_G + 512), 1u, __ATOMIC_RELAXED, __HIP_MEMORY_SCOPE_WORKGROUP); \
            if (lane == 0 && prev == 5) set_flag(p.ws + WS_FKV + (rb * 8 + g) * 128, tag); } while (0)
template <int J0>
__device__ __forceinline__ void attn_wave(const Params& p, LAS char* lds, int w, int lane, int rb, int g, u64 tag) {
    const int head = w & 3, h = lane >> 5, r31 = lane & 31;
    AttnState st;
    { const LAS char* qp = lds + L_Q + head * 6144 + (32 * J0 + r31) * 48;
      st.qA0 = *(const LAS bf16x8*)(qp + 16 * h); st.qB0 = *(const LAS bf16x8*)(qp + 64 * 48 + 16 * h);
      unsigned ta = *(const LAS unsigned*)(qp + 32), tb = *(const LAS unsigned*)(qp + 64 * 48 + 32);
      st.qA1 = __builtin_bit_cast(bf16x4, u32x2{h ? 0u : ta, 0u}); st.qB1 = __builtin_bit_cast(bf16x4, u32x2{h ? 0u : tb, 0u}); }
    float rbig = ((float)(r31 - 4 * h) + 0.5f) * 1.2676506002282294e30f;
    st.mD = make_mask<false>(rbig);
    st.oA = f32x16{}; st.oB = f32x16{};
    st.kp = lds + L_K + head * 18432 + r31 * 48 + 16 * h;
    st.k1p = lds + L_K + head * 18432 + r31 * 48 + 32;
    st.vp = lds + L_V + (r31 < 18 ? head * 18 + r31 : 72) * VROW + 16 * h;
    attn_first<J0 + 10, M_NONE, M_DIAG>(st);
    if (J0 == 0) KV_ARRIVE();
    attn_step<J0 + 10, false, true, M_NONE, M_ZERO>(st);
    attn_step<J0 + 9, false, true, M_DIAG, M_ZERO>(st);
    if (J0) { attn_step<9, true, true, M_ZERO, M_ZERO>(st); attn_step<8, true, true, M_NONE, M_NONE>(st); }
    else attn_step<8, true, true, M_NONE, M_NONE>(st);
    STAMP(3);
    BAR();
    halo_first(p, lds, (w << 6) + lane, rb, g);
    BAR();
    STAMP(4);
    if (rb >= 1) {
        attn_first<7, M_ZERO, M_ZERO>(st);
        attn_step<7, true, true, M_ZERO, M_ZERO>(st);
        attn_step<6, true, true, M_ZERO, M_ZERO>(st);
        attn_step<5, true, true, M_ZERO, M_ZERO>(st);
        attn_step<4, true, true, M_NONE, M_NONE>(st);
    }
    BAR();
    STAMP(5);
    if (rb >= 2) {
        asm volatile("" : "+v"(rbig));
        st.mD = make_mask<true>(rbig);
        if (J0 == 0) { attn_first<3, M_ZERO, M_ZERO>(st); attn_step<3, true, true, M_ZERO, M_TAIL>(st); }
        else attn_first<3, M_ZERO, M_TAIL>(st);
        attn_step<J0 + 2, true, true, M_ZERO, M_NONE>(st);
        attn_step<J0 + 1, true, false, M_TAIL, M_NONE>(st);
        attn_step<J0, true, false, M_NONE, M_NONE>(st);
    }
    float lA = swap_lo(st.oA[10]), lB = swap_lo(st.oB[10]);
    if (__builtin_expect(__any(!(lA >= 9.3e-10f && lA <= 1.07e9f && lB >= 9.3e-10f && lB <= 1.07e9f)) || FORCE_FB, 0)) {
#pragma unroll 1
        for (int it = 0; it < 2; ++it) {
            const int j = J0 + 2 * it, J = 4 * rb + j, nb = J < 8 ? J : 8;
            const f32x16 o = attn_slow(st.kp, st.vp, it ? st.qB0 : st.qA0, it ? st.qB1 : st.qA1, 8 + j - nb, 8 + j, J >= 8, lane, rbig);
            if (it) st.oB = o; else st.oA = o;
        }
        lA = swap_lo(st.oA[10]); lB = swap_lo(st.oB[10]);
    }
    const float gA = gate_at(lds, 32 * J0 + r31), gB = gate_at(lds, 32 * J0 + 64 + r31);
    write_o(lds + L_Q, lane, head, 32 * J0, st.oA, (1.0f - gA) * __builtin_amdgcn_rcpf(lA));
    write_o(lds + L_Q, lane, head, 32 * J0 + 64, st.oB, (1.0f - gB) * __builtin_amdgcn_rcpf(lB));
}

__device__ __forceinline__ void features2(const LAS char* rowp, int stride, int lane, bf16x8 wf0, bf16x4 wf1, unsigned (&pk)[2][9]) {
    const int h = lane >> 5;
    f32x16 d[2];
#pragma unroll
    for (int b = 0; b < 2; ++b) {
        const bf16x8 b0 = *(const LAS bf16x8*)(rowp + b * stride + 16 * h);
        const unsigned t = *(const LAS unsigned*)(rowp + b * stride + 32);
        d[b] = mfma32k8(wf1, __builtin_bit_cast(bf16x4, u32x2{h ? 0u : t, 0u}), mfma32(wf0, b0, f32x16{}));
    }
    float ep[2][9], en[2][9], sp[2], sn[2];
#pragma unroll
    for (int i = 0; i < 9; ++i)
#pragma unroll
        for (int b = 0; b < 2; ++b) { ep[b][i] = __builtin_amdgcn_exp2f(d[b][i]); en[b][i] = __builtin_amdgcn_exp2f(-d[b][i]); }
#define SUM9(e) (((e[0] + e[1]) + (e[2] + e[3])) + ((e[4] + e[5]) + (e[6] + e[7])) + e[8])
#pragma unroll
    for (int b = 0; b < 2; ++b) { sp[b] = SUM9(ep[b]); sn[b] = SUM9(en[b]); }
#pragma unroll
    for (int b = 0; b < 2; ++b) { sp[b] = swap_sum(sp[b]); sn[b] = swap_sum(sn[b]); }
    constexpr float BIG = 1.152921504606847e18f;
    if (__builtin_expect(__any(!(sp[0] < BIG && sn[0] < BIG && sp[1] < BIG && sn[1] < BIG)) || FORCE_FB, 0)) {
#pragma unroll
        for (int b = 0; b < 2; ++b) {
            float mx = d[b][0], mn = d[b][0];
#pragma unroll
            for (int i = 1; i < 9; ++i) { mx = fmaxf(mx, d[b][i]); mn = fminf(mn, d[b][i]); }
            mx = swap_max(mx); mn = swap_min(mn);
#pragma unroll
            for (int i = 0; i < 9; ++i) { ep[b][i] = __builtin_amdgcn_exp2f(d[b][i] - mx); en[b][i] = __builtin_amdgcn_exp2f(mn - d[b][i]); }
            sp[b] = swap_sum(SUM9(ep[b])); sn[b] = swap_sum(SUM9(en[b]));
        }
    }
#undef SUM9
#pragma unroll
    for (int b = 0; b < 2; ++b) { sp[b] = __builtin_amdgcn_rcpf(sp[b]); sn[b] = __builtin_amdgcn_rcpf(sn[b]); }
#pragma unroll
    for (int b = 0; b < 2; ++b) {
        float v[18];
#pragma unroll
        for (int i = 0; i < 9; ++i) { v[i] = ep[b][i] * sp[b]; v[9 + i] = en[b][i] * sn[b]; }
#pragma unroll
        for (int i = 0; i < 9; ++i) pk[b][i] = pack2(v[2 * i], v[2 * i + 1]);
    }
}

constexpr int P3_R = 76, P3_GC = P3_R * 9, P3_GB = 11008, P3_OQ = 4 * P3_GB,
              P3_W0 = 2 * P3_OQ, P3_WQ = 36864, P3_END = P3_W0 + 2 * P3_WQ;
constexpr int L_RDY = P3_END;
static_assert(P3_END + 256 <= LDS_BYTES, "phase 3 LDS map");
__device__ __forceinline__ void lds_wait(LAS char* lds, int off) {
    for (unsigned spins = 0; spins < (1u << 22); ++spins) {
        if (__builtin_amdgcn_readfirstlane(__hip_atomic_load((LAS unsigned*)(lds + off), __ATOMIC_RELAXED, __HIP_MEMORY_SCOPE_WORKGROUP))) break;
        __builtin_amdgcn_s_sleep(1);
    }
    asm volatile("" ::: "memory");
}
#define P3EXIT 0
#define LST_DECL
#define LST(i)
#define LST_OUT(slot)
#define PBAR(n) do { BAR(); if (P3EXIT == (n)) return; } while (0)
#define PBARC(n) do { BAR(); if (P3EXIT == (n)) { for (int a_ = 0; a_ < 5; ++a_) asm volatile("" :: "v"(acc[a_])); return; } } while (0)
template <bool LATE>
__device__ __forceinline__ void phase3(const Params& p, LAS char* lds, int tid, int lane, int w, int t0, int ct, u64 tag) {
    if (w == 9) {
        const int rb0 = t0 >> 7, rb1 = (t0 + P3_R - 1 < S ? t0 + P3_R - 1 : S - 1) >> 7;
        const char* fl = p.ws + WS_FO + (((lane & 8) ? rb1 : rb0) * 16 + (lane & 7)) * 128;
        LST_DECL; LST(0);
        wait_flags(fl, lane < 16, tag, p.ws);
        if (lane == 0) __hip_atomic_store((LAS unsigned*)(lds + L_RDY), 1u, __ATOMIC_RELAXED, __HIP_MEMORY_SCOPE_WORKGROUP);
        LST(1);
        wait_flags(fl + 8 * 128, lane < 16, tag, p.ws);
        if (lane == 0) __hip_atomic_store((LAS unsigned*)(lds + L_RDY + 4), 1u, __ATOMIC_RELAXED, __HIP_MEMORY_SCOPE_WORKGROUP);
        LST(2);
        LST_OUT(0);
        asm volatile("s_waitcnt lgkmcnt(0) vmcnt(0)" ::: "memory");
        __builtin_amdgcn_endpgm();
    } else if (w == 8) {
        const unsigned lbase = (unsigned)(size_t)lds;
        const __amdgpu_buffer_rsrc_t rwa = __builtin_amdgcn_make_buffer_rsrc((void*)p.woA, 0, D * D * 2, 0x00020000);
        const __amdgpu_buffer_rsrc_t rwb = __builtin_amdgcn_make_buffer_rsrc((void*)p.woB, 0, D * D * 2, 0x00020000);
        const int wrow = lane >> 3;
        const unsigned vwo0 = wrow * 1152 + ct * 128 + (((lane & 7) ^ (((wrow >> 1) & 1) << 1)) << 4), vwo1 = vwo0 ^ 64;
        auto issue_w = [&](int q) {
            const unsigned lb = lbase + P3_W0 + (q & 1) * P3_WQ, so = (q & 1) * 288 * 1152;
#pragma unroll 2
            for (int u = 0; u < 36; ++u) glds16(lb + u * 1024, (u & 1) ? vwo1 : vwo0, q < 2 ? rwa : rwb, so + u * 8 * 1152);
        };
        __builtin_amdgcn_s_setprio(3);
        issue_w(0); issue_w(1); WAITV(36); PBAR(1);
        WAITV(0); PBAR(2);
        issue_w(2); WAITV(0); PBAR(3);
        issue_w(3); WAITV(0); PBAR(4);
    } else if (w >= 4) {
        const __amdgpu_buffer_rsrc_t ro = __builtin_amdgcn_make_buffer_rsrc((void*)(p.ws + WS_O), 0, 16 * 2048 * 144, 0x00020000);
        const int t4 = tid - 256;
        unsigned oo[11], ol[11];
#pragma unroll
        for (int j = 0; j < 11; ++j) { const int c = t4 + 256 * j, sl = (c * 6133) >> 22, wi = c - P3_GC * sl;
            oo[j] = c < 4 * P3_GC ? (sl * 2048 + t0) * 144 + wi * 16 : 0xC0000000u; ol[j] = c < 4 * P3_GC ? sl * P3_GB + wi * 16 : P3_END + 128 + (lane & 7) * 16; }
        u32x4 x[11], y[11];
        auto load_o = [&](u32x4 (&r)[11], int q) {
#pragma unroll
            for (int j = 0; j < 11; ++j) r[j] = __builtin_amdgcn_raw_buffer_load_b128(ro, oo[j], q * (4 * 2048 * 144), O_AUX);
            __builtin_amdgcn_sched_barrier(0);
        };
        auto write_ob = [&](const u32x4 (&r)[11], int buf) {
#pragma unroll
            for (int j = 0; j < 10; ++j) *(LAS u32x4*)(lds + buf * P3_OQ + ol[j]) = r[j];
            *(LAS u32x4*)(lds + (t4 + 2560 < 4 * P3_GC ? buf * P3_OQ : 0) + ol[10]) = r[10];
            __builtin_amdgcn_sched_barrier(0);
        };
        LST_DECL; LST(0);
        lds_wait(lds, L_RDY);
        load_o(x, 0); load_o(y, 1);
        LST(1);
        write_ob(x, 0);
        LST(2);
        PBAR(1);
        if (LATE) { lds_wait(lds, L_RDY + 4); load_o(x, 2); }
        LST(3);
        write_ob(y, 1);
        if (LATE) load_o(y, 3);
        LST(4);
        PBAR(2);
        if (!LATE) { lds_wait(lds, L_RDY + 4); load_o(x, 2); load_o(y, 3); }
        LST(5);
        write_ob(x, 0);
        LST(6);
        PBAR(3);
        write_ob(y, 1);
        LST(7);
        PBAR(4);
        if (w == 4) LST_OUT(2);
    } else {
        const int fg = lane >> 4, l15 = lane & 15;
        const int fl2 = ((l15 >> 3) & 1) | ((fg & 1) << 1);
        const unsigned wl0 = P3_W0 + (72 * fg + (l15 >> 2)) * 128 + ((w ^ fl2) << 5) + 8 * (lane & 3);
        const unsigned ofb = fg * P3_GB + l15 * 144;
        f32x4 acc[5];
#pragma unroll
        for (int a = 0; a < 5; ++a) acc[a] = f32x4{0, 0, 0, 0};
        struct F3 { bf16x8 w, o[5]; };
        auto compute = [&](int qodd) {
            const unsigned we = wl0 + qodd * P3_WQ, wo_ = we ^ 64, ob = ofb + qodd * P3_OQ;
            auto rd = [&](F3& f, int j) { const unsigned wa = ((j & 1) ? wo_ : we) + j * 1024;
                f.w = cat(trread(lds + wa), trread(lds + wa + 512));
#pragma unroll
                for (int a = 0; a < 5; ++a) f.o[a] = *(const LAS bf16x8*)(lds + ob + j * 16 + a * 16 * 144); };
            auto mm = [&](const F3& f) {
#pragma unroll
                for (int a = 0; a < 5; ++a) acc[a] = mfma16(f.w, f.o[a], acc[a]); };
            F3 f0, f1, f2;
            rd(f0, 0); rd(f1, 1);
            __builtin_amdgcn_sched_barrier(0);
#define P3STEP(FA, FC, j) do { if ((j) + 2 < 9) rd(FC, (j) + 2); mm(FA); \
            __builtin_amdgcn_sched_group_barrier(0x100, 2, 0); __builtin_amdgcn_sched_group_barrier(0x008, 1, 0); __builtin_amdgcn_sched_group_barrier(0x100, 2, 0); __builtin_amdgcn_sched_group_barrier(0x008, 1, 0); \
            __builtin_amdgcn_sched_group_barrier(0x100, 1, 0); __builtin_amdgcn_sched_group_barrier(0x008, 1, 0); __builtin_amdgcn_sched_group_barrier(0x100, 2, 0); __builtin_amdgcn_sched_group_barrier(0x008, 2, 0); \
            __builtin_amdgcn_sched_barrier(0); } while (0)
            P3STEP(f0, f2, 0); P3STEP(f1, f0, 1); P3STEP(f2, f1, 2); P3STEP(f0, f2, 3); P3STEP(f1, f0, 4); P3STEP(f2, f1, 5); P3STEP(f0, f2, 6); P3STEP(f1, f0, 7); P3STEP(f2, f1, 8);
#undef P3STEP
        };
        LST_DECL;
#pragma unroll 1
        for (int hf = 0; hf < 2; ++hf) {
            PBARC(1 + 2 * hf);
            compute(0);
            PBARC(2 + 2 * hf);
            compute(1);
        }
        if (P3EXIT == 5) { for (int a_ = 0; a_ < 5; ++a_) asm volatile("" :: "v"(acc[a_])); return; }
        const __amdgpu_buffer_rsrc_t rout = __builtin_amdgcn_make_buffer_rsrc(p.out, 0, S * D * 2, 0x00020000);
#pragma unroll
        for (int a = 0; a < 5; ++a) { const int tr = 16 * a + l15, trow = t0 + tr;
            __builtin_amdgcn_raw_buffer_store_b64(u32x2{pack2(acc[a][0], acc[a][1]), pack2(acc[a][2], acc[a][3])}, rout,
                                                  (tr < P3_R && trow < S) ? trow * 1152 + ct * 128 + w * 32 + fg * 8 : 0xFFFFFF00u, 0, OUT_AUX); }
        LST(7);
        if (w == 0) LST_OUT(1);
    }
}

__global__ __launch_bounds__(640, 3) void k_fused(Params p) {
    extern __shared__ __attribute__((aligned(16))) char smem[];
    LAS char* lds = (LAS char*)smem;
    const int tid = threadIdx.x, lane = tid & 63, w = __builtin_amdgcn_readfirstlane(tid >> 6);
    const int bid = blockIdx.x, g = bid & 7, idx = bid >> 3, br = idx & 1, rb = idx >> 1, r0 = rb * 128;
    const int s0 = (rb * 5 + g * 2 + br * 4) % 9;
    STAMP_INIT();
    STAMP(0);
    u64 tag;
    const __amdgpu_buffer_rsrc_t rws = __builtin_amdgcn_make_buffer_rsrc((void*)p.ws, 0, 16 << 20, 0x00020000);
    if (w >= 8) {
        if (w == 8 && lane == 0) *(LAS unsigned*)(lds + L_G + 512) = 0u;
        tag = (w == 8) ? load_loop<0>(p, lds, lane, g, br, r0, s0) : load_loop<1>(p, lds, lane, g, br, r0, s0);
        if (EXIT_AT == 1 || EXIT_AT == 20) { WAITV(0); return; }
        BAR();
        if (w == 8 && lane < 49) *(LAS u32x4*)(lds + L_V + 72 * VROW + lane * 16) = u32x4{0x3F803F80u, 0x3F803F80u, 0x3F803F80u, 0x3F803F80u};
        if (!br) {
            const int r31 = lane & 31, h = lane >> 5, hh = (r31 >> 2) & 1, i = (r31 & 3) + 4 * (r31 >> 3); const bool valid = i <= 8 && r31 < 24; const int f = valid ? 9 * hh + i : 0;
            const unsigned m = valid ? 0xFFFFFFFFu : 0u;
            unsigned v[2][10];
#pragma unroll
            for (int e = 0; e < 2; ++e) { const LAS unsigned short* wp = (const LAS unsigned short*)(lds + L_WPHI) + (2 * (w - 8) + e) * 324 + f;
#pragma unroll
                for (int j = 0; j < 8; ++j) v[e][j] = wp[(8 * h + j + 2 * e) * 18];
                v[e][8] = wp[(e ? 0 : 16) * 18]; v[e][9] = wp[(e ? 1 : 17) * 18]; }
#pragma unroll
            for (int e = 0; e < 2; ++e) { LAS char* dst = lds + L_K + (2 * (w - 8) + e) * 18432 + 10240;
                *(LAS u32x4*)(dst + lane * 16) = u32x4{(v[e][0] | v[e][1] << 16) & m, (v[e][2] | v[e][3] << 16) & m, (v[e][4] | v[e][5] << 16) & m, (v[e][6] | v[e][7] << 16) & m};
                *(LAS u32x2*)(dst + 1024 + lane * 8) = u32x2{h ? 0u : (v[e][8] | v[e][9] << 16) & m, 0u}; }
        }
    } else {
        tag = *(const __attribute__((address_space(4))) u64*)(p.ws + WS_EPOCH) + 1;
        asm volatile("" :: "s"(tag));
        f32x4 acc[4][4];
#pragma unroll
        for (int t = 0; t < 4; ++t)
#pragma unroll
            for (int n = 0; n < 4; ++n) acc[t][n] = f32x4{0, 0, 0, 0};
        unsigned a0[4], a1[4];
#define RUN(V) do { image_addr<V>(w, lane, a0, a1); gemm_loop<V>(lds, w, lane, s0, acc); __builtin_amdgcn_s_setprio(0); if (EXIT_AT == 20) { SPAN_END(); return; } STAMP(1); if (EXIT_AT == 1) { for (int t_ = 0; t_ < 4; ++t_) for (int n_ = 0; n_ < 4; ++n_) asm volatile("" :: "v"(acc[t_][n_])); SPAN_END(); return; } BAR(); write_images<V>(lds, w, lane, br, a0, a1, acc); } while (0)
        const int v = ((w >> 1) & 1) * 2 + (w >> 2);
        if (w >= 4) __builtin_amdgcn_s_setprio(1);
        if (v == 0) RUN(0); else if (v == 1) RUN(1); else if (v == 2) RUN(2); else RUN(3);
#undef RUN
    }
    BAR();
    STAMP(2);
    if (EXIT_AT == 2) { SPAN_END(); return; }
    if (EXIT_AT == 8 && !br) { SPAN_END(); return; }
    if (EXIT_AT >= 9 && br) { SPAN_END(); return; }
    const int head = w & 3, h = lane >> 5, r31 = lane & 31;
    LAS char* ob = lds + L_Q;
    if (br) {
        if (rb < 15 && (w < 4 || w >= 8)) {
            const unsigned kg = (unsigned)WS_KG + (rb * 8 + g) * KG_BYTES; const int t6 = (w < 4 ? w : w - 4) * 64 + lane;
            u32x4 pv[7];
#pragma unroll
            for (int j = 0; j < 4; ++j) pv[j] = *(const LAS u32x4*)(lds + L_K + j * 18432 + 12288 + t6 * 16);
#pragma unroll
            for (int j = 0; j < 3; ++j) { const int cc = t6 + 384 * j; pv[4 + j] = *(const LAS u32x4*)(lds + L_V + (cc >> 4) * VROW + 512 + (cc & 15) * 16); }
#pragma unroll
            for (int j = 0; j < 7; ++j) __builtin_amdgcn_raw_buffer_store_b128(pv[j], rws, kg + (t6 + 384 * j) * 16, 0, 16);
        }
        if (w >= 8) {
            const int t2 = tid - 512;
            KV_ARRIVE();
            if (rb > 0) wait_flags(p.ws + WS_FKV + ((rb - 1 - (lane & 1)) * 8 + g) * 128, lane < 2 && rb - 1 - lane >= 0, tag, p.ws);
            BAR();
            halo_first(p, lds, tid, rb, g);
            BAR();
            u32x4 v[21];
            const unsigned src = (unsigned)WS_KG + ((rb >= 2 ? rb - 2 : 0) * 8 + g) * KG_BYTES + t2 * 16;
#pragma unroll
            for (int j = 0; j < 21; ++j) v[j] = __builtin_amdgcn_raw_buffer_load_b128(rws, rb >= 2 ? src + j * 2048 : 0xFFFFFF00u, 0, 16);
            __builtin_amdgcn_sched_barrier(0);
#pragma unroll
            for (int j = 0; j < 12; ++j) { const int c = t2 + 128 * j, hd = j / 3, wi = c - 384 * hd;
                *(LAS u32x4*)(lds + L_K + hd * 18432 + wi * 16) = v[j]; }
#pragma unroll
            for (int j = 0; j < 9; ++j) { const int c = t2 + 128 * j;
                *(LAS u32x4*)(lds + L_V + (c >> 4) * VROW + (c & 15) * 16) = v[12 + j]; }
            BAR();
        } else {
            if (w >= 4) __builtin_amdgcn_s_setprio(1);
            if (w < 4) attn_wave<0>(p, lds, w, lane, rb, g, tag); else attn_wave<1>(p, lds, w, lane, rb, g, tag);
            __builtin_amdgcn_s_setprio(0);
        }
    } else {
        const int fg = lane >> 4, l15 = lane & 15, th = (w >> 2) & 1;
        LAS char* kvb = lds + L_V;
        LAS char* pt = lds + L_K + head * 18432;
        unsigned pq[2][9];
        f32x4 kv[3][2];
        bf16x8 wf0; bf16x4 wf1;
#define AST(i)
        if (w < 8) {
            if (w >= 4) __builtin_amdgcn_s_setprio(1);
            wf0 = *(const LAS bf16x8*)(pt + 10240 + lane * 16); wf1 = *(const LAS bf16x4*)(pt + 11264 + lane * 8);
            unsigned pk[2][9];
            features2(lds + L_K + head * 18432 + (256 + 64 * th + r31) * 48, 32 * 48, lane, wf0, wf1, pk);
            AST(1);
            const LAS char* vb0p = lds + L_V + (head * 18 + l15) * VROW + (256 + 64 * th + 8 * fg) * 2;
            const LAS char* vb1p = lds + L_V + (l15 < 2 ? head * 18 + 16 + l15 : 72) * VROW + (256 + 64 * th + 8 * fg) * 2;
            bf16x8 vb0[2], vb1[2];
#pragma unroll
            for (int tb = 0; tb < 2; ++tb) { vb0[tb] = *(const LAS bf16x8*)(vb0p + tb * 64); vb1[tb] = *(const LAS bf16x8*)(vb1p + tb * 64); }
#pragma unroll
            for (int tb = 0; tb < 2; ++tb) {
                LAS char* wr = pt + (2 * th + tb) * 2560 + r31 * 80 + 40 * h;
                *(LAS u32x2*)(wr) = u32x2{pk[tb][0], pk[tb][1]}; *(LAS u32x2*)(wr + 8) = u32x2{pk[tb][2], pk[tb][3]}; *(LAS u32x2*)(wr + 16) = u32x2{pk[tb][4], pk[tb][5]};
                *(LAS u32x2*)(wr + 24) = u32x2{pk[tb][6], pk[tb][7]}; *(LAS unsigned*)(wr + 32) = pk[tb][8];
            }
            const LAS char* tr = pt + 2 * th * 2560 + (16 * (fg >> 1) + 4 * (fg & 1) + (l15 >> 2)) * 80 + 8 * (lane & 3);
            bf16x8 af[2][3];
#pragma unroll
            for (int tb = 0; tb < 2; ++tb)
#pragma unroll
                for (int ft = 0; ft < 3; ++ft) af[tb][ft] = cat(trread(tr + tb * 2560 + ft * 32), trread(tr + tb * 2560 + ft * 32 + 8 * 80));
#pragma unroll
            for (int ft = 0; ft < 3; ++ft) {
                kv[ft][0] = mfma16(af[1][ft], vb0[1], mfma16(af[0][ft], vb0[0], f32x4{0, 0, 0, 0}));
                kv[ft][1] = mfma16(af[1][ft], vb1[1], mfma16(af[0][ft], vb1[0], f32x4{0, 0, 0, 0}));
            }
            AST(2);
            if (th == 1) {
#pragma unroll
                for (int ft = 0; ft < 3; ++ft) { *(LAS f32x4*)(pt + 5120 + (2 * ft) * 1024 + lane * 16) = kv[ft][0]; *(LAS f32x4*)(pt + 5120 + (2 * ft + 1) * 1024 + lane * 16) = kv[ft][1]; }
            }
            __builtin_amdgcn_s_setprio(0);
        }
        BAR();
        AST(3);
        if (EXIT_AT == 10) { asm volatile("" :: "v"(kv[0][0]), "v"(kv[1][0]), "v"(kv[2][0]), "v"(kv[0][1]), "v"(kv[1][1]), "v"(kv[2][1])); return; }
        if (w < 4) {
            f32x4 u[3][2];
#pragma unroll
            for (int ft = 0; ft < 3; ++ft) { u[ft][0] = *(const LAS f32x4*)(pt + 5120 + (2 * ft) * 1024 + lane * 16); u[ft][1] = *(const LAS f32x4*)(pt + 5120 + (2 * ft + 1) * 1024 + lane * 16); }
            LAS char* kr = pt;
#pragma unroll
            for (int ft = 0; ft < 3; ++ft)
#pragma unroll
                for (int dt = 0; dt < 2; ++dt) { const int drow = 16 * dt + l15, f = 16 * ft + 4 * fg; const f32x4 t = kv[ft][dt] + u[ft][dt];
                    *(LAS u32x2*)(kr + ((drow < 19 && f < 40) ? drow * 80 + f * 2 : 2048 + lane * 8)) = u32x2{pack2(t[0], t[1]), pack2(t[2], t[3])}; }
            const unsigned kvp = (unsigned)WS_KVP + (g * 16 + rb) * KVP_BYTES + head * 1520;
#pragma unroll
            for (int j = 0; j < 2; ++j) { const int c = lane + 64 * j;
                __builtin_amdgcn_raw_buffer_store_b128(*(const LAS u32x4*)(kr + c * 16), rws, c < 95 ? kvp + c * 16 : 0xFFFFFF00u, 0, 16); }
        }
        AST(4);
        if (w < 8) features2(lds + L_Q + head * 6144 + (64 * th + r31) * 48, 32 * 48, lane, wf0, wf1, pq);
        AST(5);
        if (w < 4) {
            WAITV(0);
            unsigned prev = 0;
            if (lane == 0) prev = __hip_atomic_fetch_add((LAS unsigned*)(lds + L_G + 512), 1u, __ATOMIC_RELAXED, __HIP_MEMORY_SCOPE_WORKGROUP);
            if (lane == 0 && prev == 3) set_flag(p.ws + WS_FKP + (rb * 8 + g) * 128, tag);
        }
        STAMP(3);
        AST(6);
        if (w == 8) wait_flags(p.ws + WS_FKP + ((lane & 15) * 8 + g) * 128, lane < 16, tag, p.ws);
        BAR();
        STAMP(4);
        AST(7);
        if (EXIT_AT == 11) { for (int a_ = 0; a_ < 2; ++a_) for (int b_ = 0; b_ < 9; ++b_) asm volatile("" :: "v"(pq[a_][b_])); return; }
        if (tid < KVP_CHUNKS) {
            u32x4 v[16];
#pragma unroll
            for (int i = 0; i < 16; ++i) v[i] = __builtin_amdgcn_raw_buffer_load_b128(rws, (unsigned)WS_KVP + (g * 16 + i) * KVP_BYTES + tid * 16, 0, 16);
            float sum[8];
#pragma unroll
            for (int e = 0; e < 8; ++e) sum[e] = 0.f;
#pragma unroll
            for (int i = 0; i < 16; ++i)
#pragma unroll
                for (int e = 0; e < 4; ++e) {
                    sum[2 * e] += __uint_as_float(v[i][e] << 16); sum[2 * e + 1] += __uint_as_float(v[i][e] & 0xffff0000u);
                }
            const int row = (tid * 13108) >> 16, f = (tid - 5 * row) * 8;
            LAS char* dst = kvb + (row / 5) * VROW + (row % 5) * 96;
            *(LAS u32x2*)(dst + (f >= 20 ? 8 + 2 * f : 2 * f)) = u32x2{pack2(sum[0], sum[1]), pack2(sum[2], sum[3])};
            *(LAS u32x2*)(dst + (f + 4 >= 20 ? 16 + 2 * f : 8 + 2 * f)) = u32x2{pack2(sum[4], sum[5]), pack2(sum[6], sum[7])};
        }
        BAR();
        STAMP(5);
        if (EXIT_AT == 12) { for (int a_ = 0; a_ < 2; ++a_) for (int b_ = 0; b_ < 9; ++b_) asm volatile("" :: "v"(pq[a_][b_])); return; }
        if (w < 8) {
            const int row = head * 19 + (r31 < 19 ? r31 : 18);
            const LAS char* ap = kvb + (row / 5) * VROW + (row % 5) * 96 + 48 * h;
            const bf16x8 a0 = *(const LAS bf16x8*)(ap), a1 = *(const LAS bf16x8*)(ap + 16);
            const bf16x4 a2 = __builtin_bit_cast(bf16x4, u32x2{*(const LAS unsigned*)(ap + 32), 0u});
            f32x16 o[2]; float gt[2];
#pragma unroll
            for (int tb = 0; tb < 2; ++tb) {
                gt[tb] = gate_at(lds, 64 * th + 32 * tb + r31);
                o[tb] = mfma32(a0, __builtin_bit_cast(bf16x8, u32x4{pq[tb][0], pq[tb][1], pq[tb][2], pq[tb][3]}), f32x16{});
                o[tb] = mfma32(a1, __builtin_bit_cast(bf16x8, u32x4{pq[tb][4], pq[tb][5], pq[tb][6], pq[tb][7]}), o[tb]);
                o[tb] = mfma32k8(a2, __builtin_bit_cast(bf16x4, u32x2{pq[tb][8], 0u}), o[tb]);
            }
#pragma unroll
            for (int tb = 0; tb < 2; ++tb) write_o(ob, lane, head, 64 * th + 32 * tb, o[tb], gt[tb] * __builtin_amdgcn_rcpf(swap_lo(o[tb][10]) + 1e-6f));
        }
    }
    BAR();
    STAMP(6);
    if (EXIT_AT == 6 || EXIT_AT == 8 || EXIT_AT == 9) { SPAN_END(); return; }
    { const unsigned od = (unsigned)WS_O + ((br * 8 + g) * 2048 + r0) * 144;
#pragma unroll
      for (int j = 0; j < 2; ++j) { const int c = tid + 640 * j;
          if (c < 1152) __builtin_amdgcn_raw_buffer_store_b128(*(const LAS u32x4*)(ob + c * 16), rws, od + c * 16, 0, O_ST_AUX); } }
    if (tid == 576) *(LAS u32x2*)(lds + L_RDY) = u32x2{0u, 0u};
    WAITV(0);
    BAR();
    if (tid == 0) set_flag(p.ws + WS_FO + (rb * 16 + br * 8 + g) * 128, tag);
    STAMP(7);
    if (EXIT_AT == 7) { SPAN_END(); return; }
    if (bid == 255 && w == 8) {
        for (unsigned spins = 0;;) {
            bool ok = true;
#pragma unroll
            for (int j = 0; j < 4; ++j) ok &= __hip_atomic_load((const GAS u64*)(p.ws + (j < 2 ? WS_FKV : WS_FKP - 16384) + (lane + 64 * j) * 128), __ATOMIC_RELAXED, __HIP_MEMORY_SCOPE_AGENT) == tag;
            if (__all(ok)) { if (lane == 0) set_flag(p.ws + WS_EPOCH, tag); break; }
            if (++spins > SPIN_CAP) break;
            __builtin_amdgcn_s_sleep(1);
        }
    }
    STAMP_DUMP();
    { const int xcd = bid & 7, ii = bid >> 3, t = xcd * 31 + ii;
      if (ii < 31 && t < 243) { phase3<P3LATE_ALL ? true : false>(p, lds, tid, lane, w, (t / 9) * P3_R, t % 9, tag); } }
}

extern "C" void kernel_launch(void* const* d_in, const int* in_sizes, int n_in, void* d_out, int out_size, void* d_ws, size_t ws_size, hipStream_t stream) {
    Params p;
    p.x = d_in[0]; p.wqkvA = d_in[1]; p.wphi = d_in[2]; p.woA = d_in[3]; p.wqkvB = d_in[5]; p.woB = d_in[6]; p.gw = d_in[7];
    p.out = d_out; p.ws = (char*)d_ws;
    hipFuncSetAttribute((const void*)k_fused, hipFuncAttributeMaxDynamicSharedMemorySize, LDS_BYTES);
    k_fused<<<256, 640, LDS_BYTES, stream>>>(p);
}
```

```cpp
#include <hip/hip_runtime.h>
#include <stdint.h>
#include <type_traits>
#define USE_DOT2 0
#define FORCE_FB 0
#ifndef EXIT_AT
#define EXIT_AT 0
#endif
#define NOGEMM 0
#define OUT_AUX 0
#define GPRIO 1
#define APRIO 1
#define XMAP 0
#define P3LATE_ALL 1
#define W1LATE 0
#define KPRIO 1
#define O_AUX 16
#define ACQ 0
#define O_ST_AUX 16
#define PFC 1
#define ROT 1

#define LAS __attribute__((address_space(3)))
#define GAS __attribute__((address_space(1)))
typedef short bf16x8 __attribute__((ext_vector_type(8)));
typedef short bf16x4 __attribute__((ext_vector_type(4)));
typedef float f32x2 __attribute__((ext_vector_type(2)));
typedef float f32x4 __attribute__((ext_vector_type(4)));
typedef float f32x16 __attribute__((ext_vector_type(16)));
typedef unsigned u32x2 __attribute__((ext_vector_type(2)));
typedef unsigned u32x4 __attribute__((ext_vector_type(4)));
typedef __bf16 bf2_t __attribute__((ext_vector_type(2)));
typedef unsigned long long u64;

constexpr int S = 2048, D = 576;
constexpr float LOG2E = 1.4426950408889634f;
constexpr float QSCALE_B = 0.23570226039551584f * LOG2E;
constexpr float NEG = -1.0e30f;

constexpr size_t WS_EPOCH = 0, WS_FKV = 4096, WS_FKP = 20480, WS_FO = 36864, WS_TMO = 69632;
constexpr size_t WS_KG = 131072;
constexpr int KG_BYTES = 43008, KG_K = 24576;
constexpr size_t WS_KVP = 6291456;
constexpr int KVP_BYTES = 6144, KVP_CHUNKS = 380;
constexpr size_t WS_O = 8388608;
constexpr size_t WS_STAMP = 16777216;

constexpr int XS = 16384, WSB = 27648, BUFS = XS + WSB;
constexpr int L_K = 0;
constexpr int L_V = 73728, VROW = 784;
constexpr int L_Q = 130960;
constexpr int L_G = 155648;
constexpr int L_GW = 158720;
constexpr int L_WPHI = 160768;
constexpr int LDS_BYTES = 163840;
constexpr unsigned SPIN_CAP = 1u << 18;

struct Params {
    const void* x; const void* wqkvA; const void* wphi; const void* woA; const void* wqkvB; const void* woB; const void* gw;
    void* out; char* ws;
};

__device__ __forceinline__ unsigned pack2(float a, float b) { f32x2 v = {a, b}; bf2_t r = __builtin_convertvector(v, bf2_t); return __builtin_bit_cast(unsigned, r); }
__device__ __forceinline__ void glds16(unsigned ldsaddr, unsigned voff, __amdgpu_buffer_rsrc_t rsrc, unsigned soff) {
    asm volatile("s_mov_b32 m0, %0\n\ts_nop 0\n\tbuffer_load_dwordx4 %1, %2, %3 offen lds" :: "s"(ldsaddr), "v"(voff), "s"(rsrc), "s"(soff) : "memory");
}
#define BAR() asm volatile("s_waitcnt lgkmcnt(0)\n\ts_barrier" ::: "memory")
#define WAITV(n) asm volatile("s_waitcnt vmcnt(" #n ")" ::: "memory")
__device__ __forceinline__ bf16x4 trread(const LAS char* p) { typedef short v4s __attribute__((ext_vector_type(4))); return __builtin_amdgcn_ds_read_tr16_b64_v4i16((LAS v4s*)p); }
__device__ __forceinline__ bf16x8 cat(bf16x4 a, bf16x4 b) { return __builtin_shufflevector(a, b, 0, 1, 2, 3, 4, 5, 6, 7); }
__device__ __forceinline__ f32x4 mfma16(bf16x8 a, bf16x8 b, f32x4 c) { return __builtin_amdgcn_mfma_f32_16x16x32_bf16(a, b, c, 0, 0, 0); }
__device__ __forceinline__ f32x16 mfma32(bf16x8 a, bf16x8 b, f32x16 c) { return __builtin_amdgcn_mfma_f32_32x32x16_bf16(a, b, c, 0, 0, 0); }
__device__ __forceinline__ f32x16 mfma32k8(bf16x4 a, bf16x4 b, f32x16 c) { return __builtin_amdgcn_mfma_f32_32x32x8bf16_1k(a, b, c, 0, 0, 0); }
__device__ __forceinline__ float swap_lo(float v) {
    auto r = __builtin_amdgcn_permlane32_swap(__float_as_uint(v), __float_as_uint(v), false, false); return __uint_as_float(r[0]); }
__device__ __forceinline__ float swap_max(float v) { auto r = __builtin_amdgcn_permlane32_swap(__float_as_uint(v), __float_as_uint(v), false, false); return fmaxf(__uint_as_float(r[0]), __uint_as_float(r[1])); }
__device__ __forceinline__ float swap_min(float v) { auto r = __builtin_amdgcn_permlane32_swap(__float_as_uint(v), __float_as_uint(v), false, false); return fminf(__uint_as_float(r[0]), __uint_as_float(r[1])); }
__device__ __forceinline__ float swap_sum(float v) { auto r = __builtin_amdgcn_permlane32_swap(__float_as_uint(v), __float_as_uint(v), false, false); return __uint_as_float(r[0]) + __uint_as_float(r[1]); }

__device__ __forceinline__ bool wait_flags(const char* p, bool active, u64 tag, char* ws) {
    for (unsigned spins = 0;;) {
        u64 v = active ? __hip_atomic_load((const GAS u64*)p, __ATOMIC_RELAXED, __HIP_MEMORY_SCOPE_AGENT) : tag;
        if (__all(v == tag)) return true;
        __builtin_amdgcn_s_sleep(1);
        if (++spins > SPIN_CAP) { __hip_atomic_store((GAS unsigned*)(ws + WS_TMO), 1u, __ATOMIC_RELAXED, __HIP_MEMORY_SCOPE_AGENT); return false; }
    }
}
__device__ __forceinline__ void set_flag(char* p, u64 tag) { __hip_atomic_store((GAS u64*)p, tag, __ATOMIC_RELAXED, __HIP_MEMORY_SCOPE_AGENT); }

#define SPAN_END() do {} while (0)
#define STAMP(i) do {} while (0)
#define STAMP9(i) do {} while (0)
#define STAMPW(i, ws_) do {} while (0)
#define STAMP_DUMP() do {} while (0)
#define STAMP_INIT() do {} while (0)

template <int V> struct WT { static constexpr int T0 = V == 0 ? 0 : V == 1 ? 4 : V == 2 ? 7 : 11, NC = (V & 1) ? 3 : 4; };
constexpr int WSEC = 64 * 144;
template <int V> struct Frags { bf16x8 x[4], w[WT<V>::NC]; };
template <int V>
__device__ __forceinline__ void read_frags(Frags<V>& f, const LAS char* xp, const LAS char* wp, const LAS char* wp4, const LAS char* gp, bool gsel) {
#pragma unroll
    for (int t = 0; t < 4; ++t) f.x[t] = *(const LAS bf16x8*)(xp + t * 2048);
#pragma unroll
    for (int n = 0; n < WT<V>::NC; ++n) {
        constexpr int T0 = WT<V>::T0; const int T = T0 + n, sec = (16 * T) / 72, off = sec * WSEC + (16 * T - 72 * sec) * 2;
        if (T == 4) f.w[n] = cat(trread(wp4), trread(wp4 + 144));
        else f.w[n] = cat(trread(wp + off), trread(wp + off + 144));
    }
    if (V == 3) { const bf16x8 gf = *(const LAS bf16x8*)(gp); const u32x4 a = __builtin_bit_cast(u32x4, f.w[2]), b = __builtin_bit_cast(u32x4, gf);
        f.w[2] = __builtin_bit_cast(bf16x8, u32x4{gsel ? b[0] : a[0], gsel ? b[1] : a[1], gsel ? b[2] : a[2], gsel ? b[3] : a[3]}); }
}
template <int V>
__device__ __forceinline__ void mma_frags(const Frags<V>& f, f32x4 (&acc)[4][4]) {
#pragma unroll
    for (int n = 0; n < WT<V>::NC; ++n)
#pragma unroll
        for (int t = 0; t < 4; ++t) acc[t][n] = (WT<V>::T0 + n <= 8) ? mfma16(f.w[n], f.x[t], acc[t][n]) : mfma16(f.x[t], f.w[n], acc[t][n]);
}
template <int V>
__device__ __forceinline__ void interleave() {
#define SG1 __builtin_amdgcn_sched_group_barrier(0x008, 1, 0); __builtin_amdgcn_sched_group_barrier(0x100, 1, 0);
    SG1 SG1 SG1 SG1 SG1 SG1 SG1 SG1 SG1 SG1
    if (V != 1) { SG1 }
    if (V == 0 || V == 2) { SG1 }
#undef SG1
}
template <int V>
__device__ __forceinline__ void gemm_loop(LAS char* lds, int w, int lane, int s0, f32x4 (&acc)[4][4]) {
    const int fg = lane >> 4, l15 = lane & 15, pp = lane & 3;
    const unsigned sw = (l15 >> 1) & 7;
    const unsigned xb0 = (64 * (w & 1) + l15) * 128 + ((fg ^ sw) << 4), xb1 = xb0 ^ 64;
    const unsigned wb = XS + (8 * fg + 2 * (l15 >> 2)) * 144 + 8 * pp;
    const unsigned wb4 = wb + (pp < 2 ? 128 : WSEC - 16);
    const LAS char* gp0 = lds + L_GW + 16 * fg;
    const LAS char* gp = gp0 + s0 * 128;
    int ks = s0;
    const bool gsel = l15 >= 8;
    unsigned cur = 0;
    Frags<V> f0, f1;
    BAR();
    if (EXIT_AT == 20) return;
    read_frags<V>(f0, lds + xb0, lds + wb, lds + wb4, gp, gsel);
#pragma unroll 1
    for (int s = 0; s < 9; ++s) {
        read_frags<V>(f1, lds + cur + xb1, lds + cur + wb + 32 * 144, lds + cur + wb4 + 32 * 144, gp + 64, gsel);
        mma_frags<V>(f0, acc);
        interleave<V>();
        __builtin_amdgcn_sched_barrier(0);
        cur = (cur == 2 * BUFS) ? 0 : cur + BUFS; ks = (ks == 8) ? 0 : ks + 1; gp = gp0 + ks * 128;
        if (s < 8) BAR();
        read_frags<V>(f0, lds + cur + xb0, lds + cur + wb, lds + cur + wb4, gp, gsel);
        mma_frags<V>(f1, acc);
        interleave<V>();
        __builtin_amdgcn_sched_barrier(0);
    }
}

template <int LW>
__device__ __forceinline__ u64 load_loop(const Params& p, LAS char* lds, int lane, int g, int br, int r0, int s0) {
    constexpr int NV = LW ? 4 : 5;
    const __amdgpu_buffer_rsrc_t rx = __builtin_amdgcn_make_buffer_rsrc((void*)p.x, 0, S * D * 2, 0x00020000);
    const __amdgpu_buffer_rsrc_t rw = __builtin_amdgcn_make_buffer_rsrc((void*)(br ? p.wqkvB : p.wqkvA), 0, D * 3 * D * 2, 0x00020000);
    const unsigned lbase = (unsigned)(size_t)lds;
    const unsigned vx = (r0 + 8 * LW + (lane >> 3)) * 1152 + (((lane & 7) ^ ((4 * LW + (lane >> 4)) & 7)) << 4);
    auto issue_x = [&](int s, unsigned buf) {
#pragma unroll
        for (int i = 0; i < 8; ++i) glds16(lbase + buf + LW * 1024 + i * 2048, vx, rx, s * 128 + i * (16 * 1152));
    };
    STAMP(8);
    const int s1 = s0 == 8 ? 0 : s0 + 1;
    issue_x(s0, 0);
    unsigned vw[NV];
#pragma unroll
    for (int j = 0; j < NV; ++j) {
        const int ci = 64 * (LW + 2 * j) + lane, rho = (ci * 7282) >> 16, jj = ci - 9 * rho;
        const int k = (rho & ~7) + ((rho & 1) << 2) + ((rho >> 1) & 3);
        vw[j] = k * 3456 + g * 144 + jj * 16;
    }
    auto issue_w = [&](int s, unsigned buf) {
#pragma unroll
        for (int sec = 0; sec < 3; ++sec)
#pragma unroll
            for (int j = 0; j < NV; ++j) glds16(lbase + buf + XS + sec * WSEC + (LW + 2 * j) * 1024, vw[j], rw, s * (64 * 3456) + sec * 1152);
    };
    issue_w(s0, 0);
    if (LW == 0) { const __amdgpu_buffer_rsrc_t rg = __builtin_amdgcn_make_buffer_rsrc((void*)p.gw, 0, D * 2, 0x00020000);
        glds16(lbase + L_GW, lane * 16, rg, 0); glds16(lbase + L_GW + 1024, lane * 16, rg, 1024); }
    else if (!br) { const __amdgpu_buffer_rsrc_t rp = __builtin_amdgcn_make_buffer_rsrc((void*)p.wphi, 0, 32 * 324 * 2, 0x00020000);
#pragma unroll
        for (int i = 0; i < 3; ++i) glds16(lbase + L_WPHI + i * 1024, lane * 16, rp, g * 2592 + i * 1024); }
    issue_x(s1, BUFS); issue_w(s1, BUFS);
    STAMP(9);
    u64 tag = *(const __attribute__((address_space(4))) u64*)(p.ws + WS_EPOCH) + 1;
    asm volatile("" : "+s"(tag));
    unsigned nxt2 = 2 * BUFS; int k2 = s1 == 8 ? 0 : s1 + 1;
#pragma unroll 1
    for (int s = 0; s < 9; ++s) {
        if (s < 8) { if (LW) WAITV(20); else WAITV(23); } else WAITV(0);
        if (s == 0) STAMP(10);
        STAMP9(s);
        BAR();
        if (EXIT_AT == 20) return tag;
        STAMP9(9 + s);
        if (s + 2 < 9) { issue_x(k2, nxt2); issue_w(k2, nxt2); }
        nxt2 = (nxt2 == 2 * BUFS) ? 0 : nxt2 + BUFS; k2 = k2 == 8 ? 0 : k2 + 1;
    }
    return tag;
}

template <int V>
__device__ __forceinline__ void image_addr(int w, int lane, unsigned (&a0)[4], unsigned (&a1)[4]) {
    const int fg = lane >> 4, l15 = lane & 15;
#pragma unroll
    for (int n = 0; n < 4; ++n) { a0[n] = 0; a1[n] = 0; }
#pragma unroll
    for (int n = 0; n < WT<V>::NC; ++n) {
        const int T = WT<V>::T0 + n;
        if (T <= 8) {
            const int G = 4 * T + fg, q4 = (G * 57) >> 9, m = G - 9 * q4, sec = q4 >> 1, hp = q4 & 1;
            const int hstride = sec ? 384 * 48 : 128 * 48;
            const int base = (sec ? L_K + 256 * 48 : L_Q) + 2 * hp * hstride + (64 * (w & 1) + l15) * 48;
            a0[n] = base + (m < 4 ? 8 * m : (m == 4 ? 32 : hstride + 8 * (m - 5)));
            a1[n] = (m == 4) ? base + hstride + 32 : a0[n] + 4;
            asm volatile("" : "+v"(a0[n])); if (T & 1) asm volatile("" : "+v"(a1[n]));
        }
    }
    if (V >= 2) { a0[3] = L_V + l15 * VROW + (256 + 64 * (w & 1) + 4 * (((fg & 1) << 1) | (fg >> 1))) * 2; asm volatile("" : "+v"(a0[3])); }
}
template <int V>
__device__ __forceinline__ void write_images(LAS char* lds, int w, int lane, int br, const unsigned (&a0)[4], const unsigned (&a1)[4], const f32x4 (&acc)[4][4]) {
    const int fg = lane >> 4, l15 = lane & 15;
    const float qs = br ? QSCALE_B : LOG2E, ks = br ? 1.0f : LOG2E;
#pragma unroll
    for (int n = 0; n < WT<V>::NC; ++n) {
        const int T = WT<V>::T0 + n;
        if (T <= 8) {
            const float sc = T < 4 ? qs : (T > 4 ? ks : (fg < 2 ? qs : ks));
#pragma unroll
            for (int tt = 0; tt < 4; ++tt) {
                const unsigned lo = pack2(acc[tt][n][0] * sc, acc[tt][n][1] * sc), hi = pack2(acc[tt][n][2] * sc, acc[tt][n][3] * sc);
                if (T & 1) { *(LAS unsigned*)(lds + a0[n] + tt * 768) = lo; *(LAS unsigned*)(lds + a1[n] + tt * 768) = hi; }
                else *(LAS u32x2*)(lds + a0[n] + tt * 768) = u32x2{lo, hi};
            }
        } else if (T < 13 || l15 < 8) {
#pragma unroll
            for (int tt = 0; tt < 4; ++tt)
                *(LAS u32x2*)(lds + a0[3] + (T - 9) * 16 * VROW + tt * 32) = u32x2{pack2(acc[tt][n][0], acc[tt][n][1]), pack2(acc[tt][n][2], acc[tt][n][3])};
        }
    }
    if (V == 3 && l15 == 8) {
#pragma unroll
        for (int tt = 0; tt < 4; ++tt) *(LAS f32x4*)(lds + L_G + (64 * (w & 1) + 16 * tt + 4 * fg) * 4) = acc[tt][2];
    }
}
__device__ __forceinline__ float gate_at(const LAS char* lds, int tok) {
    return __builtin_amdgcn_rcpf(1.0f + __builtin_amdgcn_exp2f(-LOG2E * *(const LAS float*)(lds + L_G + tok * 4)));
}

__device__ __forceinline__ void write_o(LAS char* ob, int lane, int head, int tok0, const f32x16& o, float scale) {
    const int h = lane >> 5, tok = tok0 + (lane & 31);
    LAS char* a = ob + tok * 144 + head * 36 + 8 * h;
    *(LAS unsigned*)(a) = pack2(o[0] * scale, o[1] * scale);
    *(LAS unsigned*)(a + 4) = pack2(o[2] * scale, o[3] * scale);
    *(LAS unsigned*)(a + 16) = pack2(o[4] * scale, o[5] * scale);
    *(LAS unsigned*)(a + 20) = pack2(o[6] * scale, o[7] * scale);
    if (h == 0) *(LAS unsigned*)(a + 32) = pack2(o[8] * scale, o[9] * scale);
}

enum { M_NONE, M_ZERO, M_DIAG, M_TAIL };
struct AttnState { f32x16 oA, oB, sA, sB, mD; bf16x8 qA0, qB0; bf16x4 qA1, qB1; const LAS char* kp; const LAS char* k1p; const LAS char* vp; };
__device__ __forceinline__ void exp_pack(const f32x16& s, bf16x8& p0, bf16x8& p1) {
    u32x4 a, b;
#pragma unroll
    for (int i = 0; i < 4; ++i) {
        a[i] = pack2(__builtin_amdgcn_exp2f(s[2 * i]), __builtin_amdgcn_exp2f(s[2 * i + 1]));
        b[i] = pack2(__builtin_amdgcn_exp2f(s[8 + 2 * i]), __builtin_amdgcn_exp2f(s[9 + 2 * i]));
    }
    p0 = __builtin_bit_cast(bf16x8, a); p1 = __builtin_bit_cast(bf16x8, b);
}
template <bool TAIL>
__device__ __forceinline__ f32x16 make_mask(float rbig) {
    f32x16 m;
#pragma unroll
    for (int i = 0; i < 16; ++i) { const float t = rbig - (float)((i & 3) + 8 * (i >> 2)) * 1.2676506002282294e30f; m[i] = __builtin_amdgcn_fmed3f(TAIL ? -t : t, 0.f, -3.0e38f); }
    return m;
}
__device__ __forceinline__ f32x16 attn_slow(const LAS char* kp, const LAS char* vp, bf16x8 q0, bf16x4 q1, int kb0, int kbd, bool tail, int lane, float rbig) {
    const int h = lane >> 5;
    float m = -3.0e38f;
    f32x16 o = {};
#pragma unroll 1
    for (int pass = 0; pass < 2; ++pass) {
#pragma unroll 1
        for (int kb = kb0; kb <= kbd; ++kb) {
            const bf16x8 k0 = *(const LAS bf16x8*)(kp + kb * 1536);
            const bf16x4 k1 = __builtin_bit_cast(bf16x4, u32x2{*(const LAS unsigned*)(kp + kb * 1536 + 32 - 16 * h), 0u});
            f32x16 c = {};
            if (kb == kbd) c = make_mask<false>(rbig); else if (tail && kb == kb0) c = make_mask<true>(rbig);
            f32x16 sc = mfma32k8(k1, q1, mfma32(k0, q0, c));
            if (pass == 0) {
#pragma unroll
                for (int i = 0; i < 16; ++i) m = fmaxf(m, sc[i]);
            } else {
#pragma unroll
                for (int i = 0; i < 16; ++i) sc[i] -= m;
                bf16x8 p0, p1; exp_pack(sc, p0, p1);
                const bf16x8 v0 = *(const LAS bf16x8*)(vp + kb * 64), v1 = *(const LAS bf16x8*)(vp + kb * 64 + 32);
                o = mfma32(v0, p0, o); o = mfma32(v1, p1, o);
            }
        }
        if (pass == 0) m = swap_max(m);
    }
    return o;
}
template <int KB, int NA, int NB>
__device__ __forceinline__ void attn_scores(const AttnState& st, f32x16& nA, f32x16& nB) {
    const bf16x8 k0 = *(const LAS bf16x8*)(st.kp + KB * 1536);
    const bf16x4 k1 = __builtin_bit_cast(bf16x4, u32x2{*(const LAS unsigned*)(st.k1p + KB * 1536), 0u});
    if (NA != M_NONE) nA = mfma32k8(k1, st.qA1, mfma32(k0, st.qA0, NA == M_ZERO ? f32x16{} : st.mD));
    if (NB != M_NONE) nB = mfma32k8(k1, st.qB1, mfma32(k0, st.qB0, NB == M_ZERO ? f32x16{} : st.mD));
}
template <int KB, int NA, int NB>
__device__ __forceinline__ void attn_first(AttnState& st) { attn_scores<KB, NA, NB>(st, st.sA, st.sB); __builtin_amdgcn_sched_barrier(0); }
template <int KB, bool A, bool B, int NA, int NB>
__device__ __forceinline__ void attn_step(AttnState& st) {
    const bf16x8 v0 = *(const LAS bf16x8*)(st.vp + KB * 64), v1 = *(const LAS bf16x8*)(st.vp + KB * 64 + 32);
    f32x16 nA, nB;
    if (NA != M_NONE || NB != M_NONE) attn_scores<(KB > 0 ? KB - 1 : 0), NA, NB>(st, nA, nB);
    bf16x8 p0, p1;
    if (A) { exp_pack(st.sA, p0, p1); st.oA = mfma32(v0, p0, st.oA); st.oA = mfma32(v1, p1, st.oA); }
    if (B) { exp_pack(st.sB, p0, p1); st.oB = mfma32(v0, p0, st.oB); st.oB = mfma32(v1, p1, st.oB); }
    if (NA != M_NONE) st.sA = nA;
    if (NB != M_NONE) st.sB = nB;
    __builtin_amdgcn_sched_group_barrier(0x100, 4, 0);
#define SGA __builtin_amdgcn_sched_group_barrier(0x400, 4, 0); __builtin_amdgcn_sched_group_barrier(0x002, 2, 0); __builtin_amdgcn_sched_group_barrier(0x008, 1, 0);
    SGA SGA SGA SGA SGA SGA SGA SGA
#undef SGA
    __builtin_amdgcn_sched_barrier(0);
}

__device__ __forceinline__ void halo_first(const Params& p, LAS char* lds, int tid, int rb, int g) {
    const __amdgpu_buffer_rsrc_t rws = __builtin_amdgcn_make_buffer_rsrc((void*)p.ws, 0, 16 << 20, 0x00020000);
    const unsigned src = (unsigned)WS_KG + ((rb >= 1 ? rb - 1 : 0) * 8 + g) * KG_BYTES;
    u32x4 v[5]; unsigned la[5];
#pragma unroll
    for (int j = 0; j < 5; ++j) { const int c = tid + 640 * j; const bool ok = (j < 4 || c < 2688) && rb >= 1;
        v[j] = __builtin_amdgcn_raw_buffer_load_b128(rws, ok ? src + c * 16 : 0xFFFFFF00u, 0, 16);
        int a;
        if (j < 2 || (j == 2 && c < 1536)) { const int hd = (c * 683) >> 18, wi = c - 384 * hd; a = L_K + hd * 18432 + 6144 + wi * 16; }
        else { const int cc = c - 1536; a = L_V + (cc >> 4) * VROW + 256 + (cc & 15) * 16; }
        la[j] = ok ? a : L_G + 1024 + (tid & 15) * 16; }
    __builtin_amdgcn_sched_barrier(0);
#pragma unroll
    for (int j = 0; j < 5; ++j) *(LAS u32x4*)(lds + la[j]) = v[j];
}
#define KV_ARRIVE() do { WAITV(0); unsigned prev = 0; \
            if (lane == 0) prev = __hip_atomic_fetch_add((LAS unsigned*)(lds + L_G + 512), 1u, __ATOMIC_RELAXED, __HIP_MEMORY_SCOPE_WORKGROUP); \
            if (lane == 0 && prev == 5) set_flag(p.ws + WS_FKV + (rb * 8 + g) * 128, tag); } while (0)
template <int J0>
__device__ __forceinline__ void attn_wave(const Params& p, LAS char* lds, int w, int lane, int rb, int g, u64 tag) {
    const int head = w & 3, h = lane >> 5, r31 = lane & 31;
#define BST(i)
#define BSTV(i)
    AttnState st;
    { const LAS char* qp = lds + L_Q + head * 6144 + (32 * J0 + r31) * 48;
      st.qA0 = *(const LAS bf16x8*)(qp + 16 * h); st.qB0 = *(const LAS bf16x8*)(qp + 64 * 48 + 16 * h);
      unsigned ta = *(const LAS unsigned*)(qp + 32), tb = *(const LAS unsigned*)(qp + 64 * 48 + 32);
      st.qA1 = __builtin_bit_cast(bf16x4, u32x2{h ? 0u : ta, 0u}); st.qB1 = __builtin_bit_cast(bf16x4, u32x2{h ? 0u : tb, 0u}); }
    float rbig = ((float)(r31 - 4 * h) + 0.5f) * 1.2676506002282294e30f;
    st.mD = make_mask<false>(rbig);
    st.oA = f32x16{}; st.oB = f32x16{};
    st.kp = lds + L_K + head * 18432 + r31 * 48 + 16 * h;
    st.k1p = lds + L_K + head * 18432 + r31 * 48 + 32;
    st.vp = lds + L_V + (r31 < 18 ? head * 18 + r31 : 72) * VROW + 16 * h;
    attn_first<J0 + 10, M_NONE, M_DIAG>(st);
    if (J0 == 0) KV_ARRIVE();
    attn_step<J0 + 10, false, true, M_NONE, M_ZERO>(st);
    attn_step<J0 + 9, false, true, M_DIAG, M_ZERO>(st);
    if (J0) { attn_step<9, true, true, M_ZERO, M_ZERO>(st); attn_step<8, true, true, M_NONE, M_NONE>(st); }
    else attn_step<8, true, true, M_NONE, M_NONE>(st);
    STAMP(3);
    BSTV(1);
    BAR();
    BST(2);
    halo_first(p, lds, (w << 6) + lane, rb, g);
    BAR();
    BST(3);
    STAMP(4);
    if (rb >= 1) {
        attn_first<7, M_ZERO, M_ZERO>(st);
        attn_step<7, true, true, M_ZERO, M_ZERO>(st);
        attn_step<6, true, true, M_ZERO, M_ZERO>(st);
        attn_step<5, true, true, M_ZERO, M_ZERO>(st);
        attn_step<4, true, true, M_NONE, M_NONE>(st);
    }
    BSTV(4);
    BAR();
    BST(5);
    STAMP(5);
    if (rb >= 2) {
        asm volatile("" : "+v"(rbig));
        st.mD = make_mask<true>(rbig);
        if (J0 == 0) { attn_first<3, M_ZERO, M_ZERO>(st); attn_step<3, true, true, M_ZERO, M_TAIL>(st); }
        else attn_first<3, M_ZERO, M_TAIL>(st);
        attn_step<J0 + 2, true, true, M_ZERO, M_NONE>(st);
        attn_step<J0 + 1, true, false, M_TAIL, M_NONE>(st);
        attn_step<J0, true, false, M_NONE, M_NONE>(st);
    }
    BSTV(6);
    float lA = swap_lo(st.oA[10]), lB = swap_lo(st.oB[10]);
    if (__builtin_expect(__any(!(lA >= 9.3e-10f && lA <= 1.07e9f && lB >= 9.3e-10f && lB <= 1.07e9f)) || FORCE_FB, 0)) {
#pragma unroll 1
        for (int it = 0; it < 2; ++it) {
            const int j = J0 + 2 * it, J = 4 * rb + j, nb = J < 8 ? J : 8;
            const f32x16 o = attn_slow(st.kp, st.vp, it ? st.qB0 : st.qA0, it ? st.qB1 : st.qA1, 8 + j - nb, 8 + j, J >= 8, lane, rbig);
            if (it) st.oB = o; else st.oA = o;
        }
        lA = swap_lo(st.oA[10]); lB = swap_lo(st.oB[10]);
    }
    const float gA = gate_at(lds, 32 * J0 + r31), gB = gate_at(lds, 32 * J0 + 64 + r31);
    write_o(lds + L_Q, lane, head, 32 * J0, st.oA, (1.0f - gA) * __builtin_amdgcn_rcpf(lA));
    write_o(lds + L_Q, lane, head, 32 * J0 + 64, st.oB, (1.0f - gB) * __builtin_amdgcn_rcpf(lB));
#undef BST
#undef BSTV
}

__device__ __forceinline__ void features2(const LAS char* rowp, int stride, int lane, bf16x8 wf0, bf16x4 wf1, unsigned (&pk)[2][9]) {
    const int h = lane >> 5;
    f32x16 d[2];
#pragma unroll
    for (int b = 0; b < 2; ++b) {
        const bf16x8 b0 = *(const LAS bf16x8*)(rowp + b * stride + 16 * h);
        const unsigned t = *(const LAS unsigned*)(rowp + b * stride + 32);
        d[b] = mfma32k8(wf1, __builtin_bit_cast(bf16x4, u32x2{h ? 0u : t, 0u}), mfma32(wf0, b0, f32x16{}));
    }
    float ep[2][9], en[2][9], sp[2], sn[2];
#pragma unroll
    for (int i = 0; i < 9; ++i)
#pragma unroll
        for (int b = 0; b < 2; ++b) { ep[b][i] = __builtin_amdgcn_exp2f(d[b][i]); en[b][i] = __builtin_amdgcn_exp2f(-d[b][i]); }
#define SUM9(e) (((e[0] + e[1]) + (e[2] + e[3])) + ((e[4] + e[5]) + (e[6] + e[7])) + e[8])
#pragma unroll
    for (int b = 0; b < 2; ++b) { sp[b] = SUM9(ep[b]); sn[b] = SUM9(en[b]); }
#pragma unroll
    for (int b = 0; b < 2; ++b) { sp[b] = swap_sum(sp[b]); sn[b] = swap_sum(sn[b]); }
    constexpr float BIG = 1.152921504606847e18f;
    if (__builtin_expect(__any(!(sp[0] < BIG && sn[0] < BIG && sp[1] < BIG && sn[1] < BIG)) || FORCE_FB, 0)) {
#pragma unroll
        for (int b = 0; b < 2; ++b) {
            float mx = d[b][0], mn = d[b][0];
#pragma unroll
            for (int i = 1; i < 9; ++i) { mx = fmaxf(mx, d[b][i]); mn = fminf(mn, d[b][i]); }
            mx = swap_max(mx); mn = swap_min(mn);
#pragma unroll
            for (int i = 0; i < 9; ++i) { ep[b][i] = __builtin_amdgcn_exp2f(d[b][i] - mx); en[b][i] = __builtin_amdgcn_exp2f(mn - d[b][i]); }
            sp[b] = swap_sum(SUM9(ep[b])); sn[b] = swap_sum(SUM9(en[b]));
        }
    }
#undef SUM9
#pragma unroll
    for (int b = 0; b < 2; ++b) { sp[b] = __builtin_amdgcn_rcpf(sp[b]); sn[b] = __builtin_amdgcn_rcpf(sn[b]); }
#pragma unroll
    for (int b = 0; b < 2; ++b) {
        float v[18];
#pragma unroll
        for (int i = 0; i < 9; ++i) { v[i] = ep[b][i] * sp[b]; v[9 + i] = en[b][i] * sn[b]; }
#pragma unroll
        for (int i = 0; i < 9; ++i) pk[b][i] = pack2(v[2 * i], v[2 * i + 1]);
    }
}

constexpr int P3_R = 76, P3_GC = P3_R * 9, P3_GB = 11008, P3_OQ = 4 * P3_GB,
              P3_W0 = 2 * P3_OQ, P3_WQ = 36864, P3_END = P3_W0 + 2 * P3_WQ;
constexpr int L_RDY = P3_END;
static_assert(P3_END + 256 <= LDS_BYTES, "phase 3 LDS map");
__device__ __forceinline__ void lds_wait(LAS char* lds, int off) {
    for (unsigned spins = 0; spins < (1u << 22); ++spins) {
        if (__builtin_amdgcn_readfirstlane(__hip_atomic_load((LAS unsigned*)(lds + off), __ATOMIC_RELAXED, __HIP_MEMORY_SCOPE_WORKGROUP))) break;
        __builtin_amdgcn_s_sleep(1);
    }
    asm volatile("" ::: "memory");
}
#define P3EXIT 0
#define LST_DECL
#define LST(i)
#define LST_OUT(slot)
#define PBAR(n) do { BAR(); if (P3EXIT == (n)) return; } while (0)
#define PBARC(n) do { BAR(); if (P3EXIT == (n)) { for (int a_ = 0; a_ < 5; ++a_) asm volatile("" :: "v"(acc[a_])); return; } } while (0)
template <bool LATE>
__device__ __forceinline__ void phase3(const Params& p, LAS char* lds, int tid, int lane, int w, int t0, int ct, u64 tag) {
    if (w == 9) {
        const int rb0 = t0 >> 7, rb1 = (t0 + P3_R - 1 < S ? t0 + P3_R - 1 : S - 1) >> 7;
        const char* fl = p.ws + WS_FO + (((lane & 8) ? rb1 : rb0) * 16 + (lane & 7)) * 128;
        LST_DECL; LST(0);
        wait_flags(fl, lane < 16, tag, p.ws);
        if (lane == 0) __hip_atomic_store((LAS unsigned*)(lds + L_RDY), 1u, __ATOMIC_RELAXED, __HIP_MEMORY_SCOPE_WORKGROUP);
        LST(1);
        wait_flags(fl + 8 * 128, lane < 16, tag, p.ws);
        if (lane == 0) __hip_atomic_store((LAS unsigned*)(lds + L_RDY + 4), 1u, __ATOMIC_RELAXED, __HIP_MEMORY_SCOPE_WORKGROUP);
        LST(2);
        LST_OUT(0);
        asm volatile("s_waitcnt lgkmcnt(0) vmcnt(0)" ::: "memory");
        __builtin_amdgcn_endpgm();
    } else if (w == 8) {
        const unsigned lbase = (unsigned)(size_t)lds;
        const __amdgpu_buffer_rsrc_t rwa = __builtin_amdgcn_make_buffer_rsrc((void*)p.woA, 0, D * D * 2, 0x00020000);
        const __amdgpu_buffer_rsrc_t rwb = __builtin_amdgcn_make_buffer_rsrc((void*)p.woB, 0, D * D * 2, 0x00020000);
        const int wrow = lane >> 3;
        const unsigned vwo0 = wrow * 1152 + ct * 128 + (((lane & 7) ^ (((wrow >> 1) & 1) << 1)) << 4), vwo1 = vwo0 ^ 64;
        auto issue_w = [&](int q) {
            const unsigned lb = lbase + P3_W0 + (q & 1) * P3_WQ, so = (q & 1) * 288 * 1152;
#pragma unroll 2
            for (int u = 0; u < 36; ++u) glds16(lb + u * 1024, (u & 1) ? vwo1 : vwo0, q < 2 ? rwa : rwb, so + u * 8 * 1152);
        };
        __builtin_amdgcn_s_setprio(3);
        issue_w(0); issue_w(1); WAITV(36); PBAR(1);
        WAITV(0); PBAR(2);
        issue_w(2); WAITV(0); PBAR(3);
        issue_w(3); WAITV(0); PBAR(4);
    } else if (w >= 4) {
        const __amdgpu_buffer_rsrc_t ro = __builtin_amdgcn_make_buffer_rsrc((void*)(p.ws + WS_O), 0, 16 * 2048 * 144, 0x00020000);
        const int t4 = tid - 256;
        unsigned oo[11], ol[11];
#pragma unroll
        for (int j = 0; j < 11; ++j) { const int c = t4 + 256 * j, sl = (c * 6133) >> 22, wi = c - P3_GC * sl;
            oo[j] = c < 4 * P3_GC ? (sl * 2048 + t0) * 144 + wi * 16 : 0xC0000000u; ol[j] = c < 4 * P3_GC ? sl * P3_GB + wi * 16 : P3_END + 128 + (lane & 7) * 16; }
        u32x4 x[11], y[11];
        auto load_o = [&](u32x4 (&r)[11], int q) {
#pragma unroll
            for (int j = 0; j < 11; ++j) r[j] = __builtin_amdgcn_raw_buffer_load_b128(ro, oo[j], q * (4 * 2048 * 144), O_AUX);
            __builtin_amdgcn_sched_barrier(0);
        };
        auto write_ob = [&](const u32x4 (&r)[11], int buf) {
#pragma unroll
            for (int j = 0; j < 10; ++j) *(LAS u32x4*)(lds + buf * P3_OQ + ol[j]) = r[j];
            *(LAS u32x4*)(lds + (t4 + 2560 < 4 * P3_GC ? buf * P3_OQ : 0) + ol[10]) = r[10];
            __builtin_amdgcn_sched_barrier(0);
        };
        LST_DECL; LST(0);
        lds_wait(lds, L_RDY);
        load_o(x, 0); load_o(y, 1);
        LST(1);
        write_ob(x, 0);
        LST(2);
        PBAR(1);
        if (LATE) { lds_wait(lds, L_RDY + 4); load_o(x, 2); }
        LST(3);
        write_ob(y, 1);
        if (LATE) load_o(y, 3);
        LST(4);
        PBAR(2);
        if (!LATE) { lds_wait(lds, L_RDY + 4); load_o(x, 2); load_o(y, 3); }
        LST(5);
        write_ob(x, 0);
        LST(6);
        PBAR(3);
        write_ob(y, 1);
        LST(7);
        PBAR(4);
        if (w == 4) LST_OUT(2);
    } else {
        const int fg = lane >> 4, l15 = lane & 15;
        const int fl2 = ((l15 >> 3) & 1) | ((fg & 1) << 1);
        const unsigned wl0 = P3_W0 + (72 * fg + (l15 >> 2)) * 128 + ((w ^ fl2) << 5) + 8 * (lane & 3);
        const unsigned ofb = fg * P3_GB + l15 * 144;
        f32x4 acc[5];
#pragma unroll
        for (int a = 0; a < 5; ++a) acc[a] = f32x4{0, 0, 0, 0};
        struct F3 { bf16x8 w, o[5]; };
        auto compute = [&](int qodd) {
            const unsigned we = wl0 + qodd * P3_WQ, wo_ = we ^ 64, ob = ofb + qodd * P3_OQ;
            auto rd = [&](F3& f, int j) { const unsigned wa = ((j & 1) ? wo_ : we) + j * 1024;
                f.w = cat(trread(lds + wa), trread(lds + wa + 512));
#pragma unroll
                for (int a = 0; a < 5; ++a) f.o[a] = *(const LAS bf16x8*)(lds + ob + j * 16 + a * 16 * 144); };
            auto mm = [&](const F3& f) {
#pragma unroll
                for (int a = 0; a < 5; ++a) acc[a] = mfma16(f.w, f.o[a], acc[a]); };
            F3 f0, f1, f2;
            rd(f0, 0); rd(f1, 1);
            __builtin_amdgcn_sched_barrier(0);
#define P3STEP(FA, FC, j) do { if ((j) + 2 < 9) rd(FC, (j) + 2); mm(FA); \
            __builtin_amdgcn_sched_group_barrier(0x100, 2, 0); __builtin_amdgcn_sched_group_barrier(0x008, 1, 0); __builtin_amdgcn_sched_group_barrier(0x100, 2, 0); __builtin_amdgcn_sched_group_barrier(0x008, 1, 0); \
            __builtin_amdgcn_sched_group_barrier(0x100, 1, 0); __builtin_amdgcn_sched_group_barrier(0x008, 1, 0); __builtin_amdgcn_sched_group_barrier(0x100, 2, 0); __builtin_amdgcn_sched_group_barrier(0x008, 2, 0); \
            __builtin_amdgcn_sched_barrier(0); } while (0)
            P3STEP(f0, f2, 0); P3STEP(f1, f0, 1); P3STEP(f2, f1, 2); P3STEP(f0, f2, 3); P3STEP(f1, f0, 4); P3STEP(f2, f1, 5); P3STEP(f0, f2, 6); P3STEP(f1, f0, 7); P3STEP(f2, f1, 8);
#undef P3STEP
        };
        LST_DECL;
#pragma unroll 1
        for (int hf = 0; hf < 2; ++hf) {
            PBARC(1 + 2 * hf);
            compute(0);
            PBARC(2 + 2 * hf);
            compute(1);
        }
        if (P3EXIT == 5) { for (int a_ = 0; a_ < 5; ++a_) asm volatile("" :: "v"(acc[a_])); return; }
        const __amdgpu_buffer_rsrc_t rout = __builtin_amdgcn_make_buffer_rsrc(p.out, 0, S * D * 2, 0x00020000);
#pragma unroll
        for (int a = 0; a < 5; ++a) { const int tr = 16 * a + l15, trow = t0 + tr;
            __builtin_amdgcn_raw_buffer_store_b64(u32x2{pack2(acc[a][0], acc[a][1]), pack2(acc[a][2], acc[a][3])}, rout,
                                                  (tr < P3_R && trow < S) ? trow * 1152 + ct * 128 + w * 32 + fg * 8 : 0xFFFFFF00u, 0, OUT_AUX); }
        LST(7);
        if (w == 0) LST_OUT(1);
    }
}

__global__ __launch_bounds__(640, 3) void k_fused(Params p) {
    extern __shared__ __attribute__((aligned(16))) char smem[];
    LAS char* lds = (LAS char*)smem;
    const int tid = threadIdx.x, lane = tid & 63, w = __builtin_amdgcn_readfirstlane(tid >> 6);
    const int bid = blockIdx.x, g = bid & 7, idx = bid >> 3, br = idx & 1, rb = idx >> 1, r0 = rb * 128;
    const int s0 = (rb * 5 + g * 2 + br * 4) % 9;
    {
        u64 pc; unsigned len;
        asm volatile("s_getpc_b64 %0\n.Lpf_here%=:\n\ts_mov_b32 %1, k_fused_code_end-.Lpf_here%=" : "=s"(pc), "=s"(len));
        const __amdgpu_buffer_rsrc_t rc = __builtin_amdgcn_make_buffer_rsrc((void*)pc, 0, len, 0x00020000);
        asm volatile("s_mov_b32 m0, %0\n\ts_nop 0\n\tbuffer_load_dword %1, %2, 0 offen lds" :: "s"((unsigned)(size_t)lds + L_G + 1024), "v"((unsigned)threadIdx.x * 128u), "s"(rc) : "memory");
    }
    STAMP_INIT();
    STAMP(0);
    u64 tag;
    const __amdgpu_buffer_rsrc_t rws = __builtin_amdgcn_make_buffer_rsrc((void*)p.ws, 0, 16 << 20, 0x00020000);
    if (__builtin_expect(w >= 8, 1)) {
        if (w == 8 && lane == 0) *(LAS unsigned*)(lds + L_G + 512) = 0u;
        tag = (w == 8) ? load_loop<0>(p, lds, lane, g, br, r0, s0) : load_loop<1>(p, lds, lane, g, br, r0, s0);
        if (EXIT_AT == 1 || EXIT_AT == 20) { WAITV(0); return; }
        BAR();
        if (w == 8 && lane < 49) *(LAS u32x4*)(lds + L_V + 72 * VROW + lane * 16) = u32x4{0x3F803F80u, 0x3F803F80u, 0x3F803F80u, 0x3F803F80u};
        if (!br) {
            const int r31 = lane & 31, h = lane >> 5, hh = (r31 >> 2) & 1, i = (r31 & 3) + 4 * (r31 >> 3); const bool valid = i <= 8 && r31 < 24; const int f = valid ? 9 * hh + i : 0;
            const unsigned m = valid ? 0xFFFFFFFFu : 0u;
            unsigned v[2][10];
#pragma unroll
            for (int e = 0; e < 2; ++e) { const LAS unsigned short* wp = (const LAS unsigned short*)(lds + L_WPHI) + (2 * (w - 8) + e) * 324 + f;
#pragma unroll
                for (int j = 0; j < 8; ++j) v[e][j] = wp[(8 * h + j + 2 * e) * 18];
                v[e][8] = wp[(e ? 0 : 16) * 18]; v[e][9] = wp[(e ? 1 : 17) * 18]; }
#pragma unroll
            for (int e = 0; e < 2; ++e) { LAS char* dst = lds + L_K + (2 * (w - 8) + e) * 18432 + 10240;
                *(LAS u32x4*)(dst + lane * 16) = u32x4{(v[e][0] | v[e][1] << 16) & m, (v[e][2] | v[e][3] << 16) & m, (v[e][4] | v[e][5] << 16) & m, (v[e][6] | v[e][7] << 16) & m};
                *(LAS u32x2*)(dst + 1024 + lane * 8) = u32x2{h ? 0u : (v[e][8] | v[e][9] << 16) & m, 0u}; }
        }
    } else {
        tag = *(const __attribute__((address_space(4))) u64*)(p.ws + WS_EPOCH) + 1;
        asm volatile("" : "+s"(tag));
        f32x4 acc[4][4];
#pragma unroll
        for (int t = 0; t < 4; ++t)
#pragma unroll
            for (int n = 0; n < 4; ++n) acc[t][n] = f32x4{0, 0, 0, 0};
        unsigned a0[4], a1[4];
#define RUN(V) do { image_addr<V>(w, lane, a0, a1); gemm_loop<V>(lds, w, lane, s0, acc); __builtin_amdgcn_s_setprio(0); if (EXIT_AT == 20) { SPAN_END(); return; } STAMP(1); if (EXIT_AT == 1) { for (int t_ = 0; t_ < 4; ++t_) for (int n_ = 0; n_ < 4; ++n_) asm volatile("" :: "v"(acc[t_][n_])); SPAN_END(); return; } BAR(); write_images<V>(lds, w, lane, br, a0, a1, acc); } while (0)
        const int v = ((w >> 1) & 1) * 2 + (w >> 2);
        if (w >= 4) __builtin_amdgcn_s_setprio(1);
        if (v == 0) RUN(0); else if (v == 1) RUN(1); else if (v == 2) RUN(2); else RUN(3);
#undef RUN
    }
    BAR();
    STAMP(2);
    if (EXIT_AT == 2) { SPAN_END(); return; }
    if (EXIT_AT == 8 && !br) { SPAN_END(); return; }
    if (EXIT_AT >= 9 && br) { SPAN_END(); return; }
    const int head = w & 3, h = lane >> 5, r31 = lane & 31;
    LAS char* ob = lds + L_Q;
    if (br) {
        if (rb < 15 && (w < 4 || w >= 8)) {
            const unsigned kg = (unsigned)WS_KG + (rb * 8 + g) * KG_BYTES; const int t6 = (w < 4 ? w : w - 4) * 64 + lane;
            u32x4 pv[7];
#pragma unroll
            for (int j = 0; j < 4; ++j) pv[j] = *(const LAS u32x4*)(lds + L_K + j * 18432 + 12288 + t6 * 16);
#pragma unroll
            for (int j = 0; j < 3; ++j) { const int cc = t6 + 384 * j; pv[4 + j] = *(const LAS u32x4*)(lds + L_V + (cc >> 4) * VROW + 512 + (cc & 15) * 16); }
#pragma unroll
            for (int j = 0; j < 7; ++j) __builtin_amdgcn_raw_buffer_store_b128(pv[j], rws, kg + (t6 + 384 * j) * 16, 0, 16);
        }
        if (w >= 8) {
            const int t2 = tid - 512;
            KV_ARRIVE();
            if (rb > 0) wait_flags(p.ws + WS_FKV + ((rb - 1 - (lane & 1)) * 8 + g) * 128, lane < 2 && rb - 1 - lane >= 0, tag, p.ws);
            BAR();
            halo_first(p, lds, tid, rb, g);
            BAR();
            u32x4 v[21];
            const unsigned src = (unsigned)WS_KG + ((rb >= 2 ? rb - 2 : 0) * 8 + g) * KG_BYTES + t2 * 16;
#pragma unroll
            for (int j = 0; j < 21; ++j) v[j] = __builtin_amdgcn_raw_buffer_load_b128(rws, rb >= 2 ? src + j * 2048 : 0xFFFFFF00u, 0, 16);
            __builtin_amdgcn_sched_barrier(0);
#pragma unroll
            for (int j = 0; j < 12; ++j) { const int c = t2 + 128 * j, hd = j / 3, wi = c - 384 * hd;
                *(LAS u32x4*)(lds + L_K + hd * 18432 + wi * 16) = v[j]; }
#pragma unroll
            for (int j = 0; j < 9; ++j) { const int c = t2 + 128 * j;
                *(LAS u32x4*)(lds + L_V + (c >> 4) * VROW + (c & 15) * 16) = v[12 + j]; }
            BAR();
        } else {
            if (w >= 4) __builtin_amdgcn_s_setprio(1);
            if (w < 4) attn_wave<0>(p, lds, w, lane, rb, g, tag); else attn_wave<1>(p, lds, w, lane, rb, g, tag);
            __builtin_amdgcn_s_setprio(0);
        }
    } else {
        const int fg = lane >> 4, l15 = lane & 15, th = (w >> 2) & 1;
        LAS char* kvb = lds + L_V;
        LAS char* pt = lds + L_K + head * 18432;
        unsigned pq[2][9];
        f32x4 kv[3][2];
        bf16x8 wf0; bf16x4 wf1;
#define AST(i)
        if (w < 8) {
            if (w >= 4) __builtin_amdgcn_s_setprio(1);
            wf0 = *(const LAS bf16x8*)(pt + 10240 + lane * 16); wf1 = *(const LAS bf16x4*)(pt + 11264 + lane * 8);
            unsigned pk[2][9];
            features2(lds + L_K + head * 18432 + (256 + 64 * th + r31) * 48, 32 * 48, lane, wf0, wf1, pk);
            AST(1);
            const LAS char* vb0p = lds + L_V + (head * 18 + l15) * VROW + (256 + 64 * th + 8 * fg) * 2;
            const LAS char* vb1p = lds + L_V + (l15 < 2 ? head * 18 + 16 + l15 : 72) * VROW + (256 + 64 * th + 8 * fg) * 2;
            bf16x8 vb0[2], vb1[2];
#pragma unroll
            for (int tb = 0; tb < 2; ++tb) { vb0[tb] = *(const LAS bf16x8*)(vb0p + tb * 64); vb1[tb] = *(const LAS bf16x8*)(vb1p + tb * 64); }
#pragma unroll
            for (int tb = 0; tb < 2; ++tb) {
                LAS char* wr = pt + (2 * th + tb) * 2560 + r31 * 80 + 40 * h;
                *(LAS u32x2*)(wr) = u32x2{pk[tb][0], pk[tb][1]}; *(LAS u32x2*)(wr + 8) = u32x2{pk[tb][2], pk[tb][3]}; *(LAS u32x2*)(wr + 16) = u32x2{pk[tb][4], pk[tb][5]};
                *(LAS u32x2*)(wr + 24) = u32x2{pk[tb][6], pk[tb][7]}; *(LAS unsigned*)(wr + 32) = pk[tb][8];
            }
            const LAS char* tr = pt + 2 * th * 2560 + (16 * (fg >> 1) + 4 * (fg & 1) + (l15 >> 2)) * 80 + 8 * (lane & 3);
            bf16x8 af[2][3];
#pragma unroll
            for (int tb = 0; tb < 2; ++tb)
#pragma unroll
                for (int ft = 0; ft < 3; ++ft) af[tb][ft] = cat(trread(tr + tb * 2560 + ft * 32), trread(tr + tb * 2560 + ft * 32 + 8 * 80));
#pragma unroll
            for (int ft = 0; ft < 3; ++ft) {
                kv[ft][0] = mfma16(af[1][ft], vb0[1], mfma16(af[0][ft], vb0[0], f32x4{0, 0, 0, 0}));
                kv[ft][1] = mfma16(af[1][ft], vb1[1], mfma16(af[0][ft], vb1[0], f32x4{0, 0, 0, 0}));
            }
            AST(2);
            if (th == 1) {
#pragma unroll
                for (int ft = 0; ft < 3; ++ft) { *(LAS f32x4*)(pt + 5120 + (2 * ft) * 1024 + lane * 16) = kv[ft][0]; *(LAS f32x4*)(pt + 5120 + (2 * ft + 1) * 1024 + lane * 16) = kv[ft][1]; }
            }
            __builtin_amdgcn_s_setprio(0);
        }
        BAR();
        AST(3);
        if (EXIT_AT == 10) { asm volatile("" :: "v"(kv[0][0]), "v"(kv[1][0]), "v"(kv[2][0]), "v"(kv[0][1]), "v"(kv[1][1]), "v"(kv[2][1])); return; }
        if (w < 4) {
            f32x4 u[3][2];
#pragma unroll
            for (int ft = 0; ft < 3; ++ft) { u[ft][0] = *(const LAS f32x4*)(pt + 5120 + (2 * ft) * 1024 + lane * 16); u[ft][1] = *(const LAS f32x4*)(pt + 5120 + (2 * ft + 1) * 1024 + lane * 16); }
            LAS char* kr = pt;
#pragma unroll
            for (int ft = 0; ft < 3; ++ft)
#pragma unroll
                for (int dt = 0; dt < 2; ++dt) { const int drow = 16 * dt + l15, f = 16 * ft + 4 * fg; const f32x4 t = kv[ft][dt] + u[ft][dt];
                    *(LAS u32x2*)(kr + ((drow < 19 && f < 40) ? drow * 80 + f * 2 : 2048 + lane * 8)) = u32x2{pack2(t[0], t[1]), pack2(t[2], t[3])}; }
            const unsigned kvp = (unsigned)WS_KVP + (g * 16 + rb) * KVP_BYTES + head * 1520;
#pragma unroll
            for (int j = 0; j < 2; ++j) { const int c = lane + 64 * j;
                __builtin_amdgcn_raw_buffer_store_b128(*(const LAS u32x4*)(kr + c * 16), rws, c < 95 ? kvp + c * 16 : 0xFFFFFF00u, 0, 16); }
        }
        AST(4);
        if (w < 8) features2(lds + L_Q + head * 6144 + (64 * th + r31) * 48, 32 * 48, lane, wf0, wf1, pq);
        AST(5);
        if (w < 4) {
            WAITV(0);
            unsigned prev = 0;
            if (lane == 0) prev = __hip_atomic_fetch_add((LAS unsigned*)(lds + L_G + 512), 1u, __ATOMIC_RELAXED, __HIP_MEMORY_SCOPE_WORKGROUP);
            if (lane == 0 && prev == 3) set_flag(p.ws + WS_FKP + (rb * 8 + g) * 128, tag);
        }
        STAMP(3);
        AST(6);
        if (w == 8) wait_flags(p.ws + WS_FKP + ((lane & 15) * 8 + g) * 128, lane < 16, tag, p.ws);
        BAR();
        STAMP(4);
        AST(7);
        if (EXIT_AT == 11) { for (int a_ = 0; a_ < 2; ++a_) for (int b_ = 0; b_ < 9; ++b_) asm volatile("" :: "v"(pq[a_][b_])); return; }
        if (tid < KVP_CHUNKS) {
            u32x4 v[16];
#pragma unroll
            for (int i = 0; i < 16; ++i) v[i] = __builtin_amdgcn_raw_buffer_load_b128(rws, (unsigned)WS_KVP + (g * 16 + i) * KVP_BYTES + tid * 16, 0, 16);
            float sum[8];
#pragma unroll
            for (int e = 0; e < 8; ++e) sum[e] = 0.f;
#pragma unroll
            for (int i = 0; i < 16; ++i)
#pragma unroll
                for (int e = 0; e < 4; ++e) {
                    sum[2 * e] += __uint_as_float(v[i][e] << 16); sum[2 * e + 1] += __uint_as_float(v[i][e] & 0xffff0000u);
                }
            const int row = (tid * 13108) >> 16, f = (tid - 5 * row) * 8;
            LAS char* dst = kvb + (row / 5) * VROW + (row % 5) * 96;
            *(LAS u32x2*)(dst + (f >= 20 ? 8 + 2 * f : 2 * f)) = u32x2{pack2(sum[0], sum[1]), pack2(sum[2], sum[3])};
            *(LAS u32x2*)(dst + (f + 4 >= 20 ? 16 + 2 * f : 8 + 2 * f)) = u32x2{pack2(sum[4], sum[5]), pack2(sum[6], sum[7])};
        }
        BAR();
        STAMP(5);
        if (EXIT_AT == 12) { for (int a_ = 0; a_ < 2; ++a_) for (int b_ = 0; b_ < 9; ++b_) asm volatile("" :: "v"(pq[a_][b_])); return; }
        if (w < 8) {
            const int row = head * 19 + (r31 < 19 ? r31 : 18);
            const LAS char* ap = kvb + (row / 5) * VROW + (row % 5) * 96 + 48 * h;
            const bf16x8 a0 = *(const LAS bf16x8*)(ap), a1 = *(const LAS bf16x8*)(ap + 16);
            const bf16x4 a2 = __builtin_bit_cast(bf16x4, u32x2{*(const LAS unsigned*)(ap + 32), 0u});
            f32x16 o[2]; float gt[2];
#pragma unroll
            for (int tb = 0; tb < 2; ++tb) {
                gt[tb] = gate_at(lds, 64 * th + 32 * tb + r31);
                o[tb] = mfma32(a0, __builtin_bit_cast(bf16x8, u32x4{pq[tb][0], pq[tb][1], pq[tb][2], pq[tb][3]}), f32x16{});
                o[tb] = mfma32(a1, __builtin_bit_cast(bf16x8, u32x4{pq[tb][4], pq[tb][5], pq[tb][6], pq[tb][7]}), o[tb]);
                o[tb] = mfma32k8(a2, __builtin_bit_cast(bf16x4, u32x2{pq[tb][8], 0u}), o[tb]);
            }
#pragma unroll
            for (int tb = 0; tb < 2; ++tb) write_o(ob, lane, head, 64 * th + 32 * tb, o[tb], gt[tb] * __builtin_amdgcn_rcpf(swap_lo(o[tb][10]) + 1e-6f));
        }
    }
    BAR();
    STAMP(6);
    if (EXIT_AT == 6 || EXIT_AT == 8 || EXIT_AT == 9) { SPAN_END(); return; }
    { const unsigned od = (unsigned)WS_O + ((br * 8 + g) * 2048 + r0) * 144;
#pragma unroll
      for (int j = 0; j < 2; ++j) { const int c = tid + 640 * j;
          if (c < 1152) __builtin_amdgcn_raw_buffer_store_b128(*(const LAS u32x4*)(ob + c * 16), rws, od + c * 16, 0, O_ST_AUX); } }
    if (tid == 576) *(LAS u32x2*)(lds + L_RDY) = u32x2{0u, 0u};
    WAITV(0);
    BAR();
    if (tid == 0) set_flag(p.ws + WS_FO + (rb * 16 + br * 8 + g) * 128, tag);
    STAMP(7);
    if (EXIT_AT == 7) { SPAN_END(); return; }
    if (bid == 255 && w == 8) {
        for (unsigned spins = 0;;) {
            bool ok = true;
#pragma unroll
            for (int j = 0; j < 4; ++j) ok &= __hip_atomic_load((const GAS u64*)(p.ws + (j < 2 ? WS_FKV : WS_FKP - 16384) + (lane + 64 * j) * 128), __ATOMIC_RELAXED, __HIP_MEMORY_SCOPE_AGENT) == tag;
            if (__all(ok)) { if (lane == 0) set_flag(p.ws + WS_EPOCH, tag); break; }
            if (++spins > SPIN_CAP) break;
            __builtin_amdgcn_s_sleep(1);
        }
    }
    STAMP_DUMP();
    { const int xcd = bid & 7, ii = bid >> 3, t = xcd * 31 + ii;
      if (ii < 31 && t < 243) { phase3<P3LATE_ALL ? true : false>(p, lds, tid, lane, w, (t / 9) * P3_R, t % 9, tag); } }
    asm volatile("k_fused_code_end:");
}

extern "C" void kernel_launch(void* const* d_in, const int* in_sizes, int n_in, void* d_out, int out_size, void* d_ws, size_t ws_size, hipStream_t stream) {
    Params p;
    p.x = d_in[0]; p.wqkvA = d_in[1]; p.wphi = d_in[2]; p.woA = d_in[3]; p.wqkvB = d_in[5]; p.woB = d_in[6]; p.gw = d_in[7];
    p.out = d_out; p.ws = (char*)d_ws;
    hipFuncSetAttribute((const void*)k_fused, hipFuncAttributeMaxDynamicSharedMemorySize, LDS_BYTES);
    k_fused<<<256, 640, LDS_BYTES, stream>>>(p);
}
```

```cpp
#include <hip/hip_runtime.h>
#include <stdint.h>
#define FORCE_FB 0

#define LAS __attribute__((address_space(3)))
#define GAS __attribute__((address_space(1)))
typedef short bf16x8 __attribute__((ext_vector_type(8)));
typedef short bf16x4 __attribute__((ext_vector_type(4)));
typedef float f32x2 __attribute__((ext_vector_type(2)));
typedef float f32x4 __attribute__((ext_vector_type(4)));
typedef float f32x16 __attribute__((ext_vector_type(16)));
typedef unsigned u32x2 __attribute__((ext_vector_type(2)));
typedef unsigned u32x4 __attribute__((ext_vector_type(4)));
typedef __bf16 bf2_t __attribute__((ext_vector_type(2)));
typedef unsigned long long u64;

constexpr int S = 2048, D = 576;
constexpr float LOG2E = 1.4426950408889634f;
constexpr float QSCALE_B = 0.23570226039551584f * LOG2E;

constexpr size_t WS_EPOCH = 0, WS_FKV = 4096, WS_FKP = 20480, WS_FO = 36864, WS_TMO = 69632;
constexpr size_t WS_KG = 131072;
constexpr int KG_BYTES = 43008;
constexpr size_t WS_KVP = 6291456;
constexpr int KVP_BYTES = 6144, KVP_CHUNKS = 380;
constexpr size_t WS_O = 8388608;

constexpr int XS = 16384, WSB = 27648, BUFS = XS + WSB;
constexpr int L_K = 0;
constexpr int L_V = 73728, VROW = 784;
constexpr int L_Q = 130960;
constexpr int L_G = 155648;
constexpr int L_GW = 158720;
constexpr int L_WPHI = 160768;
constexpr int LDS_BYTES = 163840;
constexpr unsigned SPIN_CAP = 1u << 18;

struct Params {
    const void* x; const void* wqkvA; const void* wphi; const void* woA; const void* wqkvB; const void* woB; const void* gw;
    void* out; char* ws;
};

__device__ __forceinline__ unsigned pack2(float a, float b) { f32x2 v = {a, b}; bf2_t r = __builtin_convertvector(v, bf2_t); return __builtin_bit_cast(unsigned, r); }
__device__ __forceinline__ void glds16(unsigned ldsaddr, unsigned voff, __amdgpu_buffer_rsrc_t rsrc, unsigned soff) {
    asm volatile("s_mov_b32 m0, %0\n\ts_nop 0\n\tbuffer_load_dwordx4 %1, %2, %3 offen lds" :: "s"(ldsaddr), "v"(voff), "s"(rsrc), "s"(soff) : "memory");
}
#define BAR() asm volatile("s_waitcnt lgkmcnt(0)\n\ts_barrier" ::: "memory")
#define WAITV(n) asm volatile("s_waitcnt vmcnt(" #n ")" ::: "memory")
__device__ __forceinline__ bf16x4 trread(const LAS char* p) { typedef short v4s __attribute__((ext_vector_type(4))); return __builtin_amdgcn_ds_read_tr16_b64_v4i16((LAS v4s*)p); }
__device__ __forceinline__ bf16x8 cat(bf16x4 a, bf16x4 b) { return __builtin_shufflevector(a, b, 0, 1, 2, 3, 4, 5, 6, 7); }
__device__ __forceinline__ f32x4 mfma16(bf16x8 a, bf16x8 b, f32x4 c) { return __builtin_amdgcn_mfma_f32_16x16x32_bf16(a, b, c, 0, 0, 0); }
__device__ __forceinline__ f32x16 mfma32(bf16x8 a, bf16x8 b, f32x16 c) { return __builtin_amdgcn_mfma_f32_32x32x16_bf16(a, b, c, 0, 0, 0); }
__device__ __forceinline__ f32x16 mfma32k8(bf16x4 a, bf16x4 b, f32x16 c) { return __builtin_amdgcn_mfma_f32_32x32x8bf16_1k(a, b, c, 0, 0, 0); }
__device__ __forceinline__ float swap_lo(float v) {
    auto r = __builtin_amdgcn_permlane32_swap(__float_as_uint(v), __float_as_uint(v), false, false); return __uint_as_float(r[0]); }
__device__ __forceinline__ float swap_max(float v) { auto r = __builtin_amdgcn_permlane32_swap(__float_as_uint(v), __float_as_uint(v), false, false); return fmaxf(__uint_as_float(r[0]), __uint_as_float(r[1])); }
__device__ __forceinline__ float swap_min(float v) { auto r = __builtin_amdgcn_permlane32_swap(__float_as_uint(v), __float_as_uint(v), false, false); return fminf(__uint_as_float(r[0]), __uint_as_float(r[1])); }
__device__ __forceinline__ float swap_sum(float v) { auto r = __builtin_amdgcn_permlane32_swap(__float_as_uint(v), __float_as_uint(v), false, false); return __uint_as_float(r[0]) + __uint_as_float(r[1]); }

__device__ __forceinline__ bool wait_flags(const char* p, bool active, u64 tag, char* ws) {
    for (unsigned spins = 0;;) {
        u64 v = active ? __hip_atomic_load((const GAS u64*)p, __ATOMIC_RELAXED, __HIP_MEMORY_SCOPE_AGENT) : tag;
        if (__all(v == tag)) return true;
        __builtin_amdgcn_s_sleep(1);
        if (++spins > SPIN_CAP) { __hip_atomic_store((GAS unsigned*)(ws + WS_TMO), 1u, __ATOMIC_RELAXED, __HIP_MEMORY_SCOPE_AGENT); return false; }
    }
}
__device__ __forceinline__ void set_flag(char* p, u64 tag) { __hip_atomic_store((GAS u64*)p, tag, __ATOMIC_RELAXED, __HIP_MEMORY_SCOPE_AGENT); }

template <int V> struct WT { static constexpr int T0 = V == 0 ? 0 : V == 1 ? 4 : V == 2 ? 7 : 11, NC = (V & 1) ? 3 : 4; };
constexpr int WSEC = 64 * 144;
template <int V> struct Frags { bf16x8 x[4], w[WT<V>::NC]; };
template <int V>
__device__ __forceinline__ void read_frags(Frags<V>& f, const LAS char* xp, const LAS char* wp, const LAS char* wp4, const LAS char* gp, bool gsel) {
#pragma unroll
    for (int t = 0; t < 4; ++t) f.x[t] = *(const LAS bf16x8*)(xp + t * 2048);
#pragma unroll
    for (int n = 0; n < WT<V>::NC; ++n) {
        constexpr int T0 = WT<V>::T0; const int T = T0 + n, sec = (16 * T) / 72, off = sec * WSEC + (16 * T - 72 * sec) * 2;
        if (T == 4) f.w[n] = cat(trread(wp4), trread(wp4 + 144));
        else f.w[n] = cat(trread(wp + off), trread(wp + off + 144));
    }
    if (V == 3) { const bf16x8 gf = *(const LAS bf16x8*)(gp); const u32x4 a = __builtin_bit_cast(u32x4, f.w[2]), b = __builtin_bit_cast(u32x4, gf);
        f.w[2] = __builtin_bit_cast(bf16x8, u32x4{gsel ? b[0] : a[0], gsel ? b[1] : a[1], gsel ? b[2] : a[2], gsel ? b[3] : a[3]}); }
}
template <int V>
__device__ __forceinline__ void mma_frags(const Frags<V>& f, f32x4 (&acc)[4][4]) {
#pragma unroll
    for (int n = 0; n < WT<V>::NC; ++n)
#pragma unroll
        for (int t = 0; t < 4; ++t) acc[t][n] = (WT<V>::T0 + n <= 8) ? mfma16(f.w[n], f.x[t], acc[t][n]) : mfma16(f.x[t], f.w[n], acc[t][n]);
}
template <int V>
__device__ __forceinline__ void interleave() {
#define SG1 __builtin_amdgcn_sched_group_barrier(0x008, 1, 0); __builtin_amdgcn_sched_group_barrier(0x100, 1, 0);
    SG1 SG1 SG1 SG1 SG1 SG1 SG1 SG1 SG1 SG1
    if (V != 1) { SG1 }
    if (V == 0 || V == 2) { SG1 }
#undef SG1
}
template <int V>
__device__ __forceinline__ void gemm_loop(LAS char* lds, int w, int lane, int s0, f32x4 (&acc)[4][4]) {
    const int fg = lane >> 4, l15 = lane & 15, pp = lane & 3;
    const unsigned sw = (l15 >> 1) & 7;
    const unsigned xb0 = (64 * (w & 1) + l15) * 128 + ((fg ^ sw) << 4), xb1 = xb0 ^ 64;
    const unsigned wb = XS + (8 * fg + 2 * (l15 >> 2)) * 144 + 8 * pp;
    const unsigned wb4 = wb + (pp < 2 ? 128 : WSEC - 16);
    const LAS char* gp0 = lds + L_GW + 16 * fg;
    const LAS char* gp = gp0 + s0 * 128;
    int ks = s0;
    const bool gsel = l15 >= 8;
    unsigned cur = 0;
    Frags<V> f0, f1;
    BAR();

    read_frags<V>(f0, lds + xb0, lds + wb, lds + wb4, gp, gsel);
#pragma unroll 1
    for (int s = 0; s < 9; ++s) {
        read_frags<V>(f1, lds + cur + xb1, lds + cur + wb + 32 * 144, lds + cur + wb4 + 32 * 144, gp + 64, gsel);
        mma_frags<V>(f0, acc);
        interleave<V>();
        __builtin_amdgcn_sched_barrier(0);
        cur = (cur == 2 * BUFS) ? 0 : cur + BUFS; ks = (ks == 8) ? 0 : ks + 1; gp = gp0 + ks * 128;
        if (s < 8) BAR();
        read_frags<V>(f0, lds + cur + xb0, lds + cur + wb, lds + cur + wb4, gp, gsel);
        mma_frags<V>(f1, acc);
        interleave<V>();
        __builtin_amdgcn_sched_barrier(0);
    }
}

template <int LW>
__device__ __forceinline__ void load_loop(const Params& p, LAS char* lds, int lane, int g, int br, int r0, int s0) {
    constexpr int NV = LW ? 4 : 5;
    const __amdgpu_buffer_rsrc_t rx = __builtin_amdgcn_make_buffer_rsrc((void*)p.x, 0, S * D * 2, 0x00020000);
    const __amdgpu_buffer_rsrc_t rw = __builtin_amdgcn_make_buffer_rsrc((void*)(br ? p.wqkvB : p.wqkvA), 0, D * 3 * D * 2, 0x00020000);
    const unsigned lbase = (unsigned)(size_t)lds;
    const unsigned vx = (r0 + 8 * LW + (lane >> 3)) * 1152 + (((lane & 7) ^ ((4 * LW + (lane >> 4)) & 7)) << 4);
    auto issue_x = [&](int s, unsigned buf) {
#pragma unroll
        for (int i = 0; i < 8; ++i) glds16(lbase + buf + LW * 1024 + i * 2048, vx, rx, s * 128 + i * (16 * 1152));
    };
    const int s1 = s0 == 8 ? 0 : s0 + 1;
    issue_x(s0, 0);
    unsigned vw[NV];
#pragma unroll
    for (int j = 0; j < NV; ++j) {
        const int ci = 64 * (LW + 2 * j) + lane, rho = (ci * 7282) >> 16, jj = ci - 9 * rho;
        const int k = (rho & ~7) + ((rho & 1) << 2) + ((rho >> 1) & 3);
        vw[j] = k * 3456 + g * 144 + jj * 16;
    }
    auto issue_w = [&](int s, unsigned buf) {
#pragma unroll
        for (int sec = 0; sec < 3; ++sec)
#pragma unroll
            for (int j = 0; j < NV; ++j) glds16(lbase + buf + XS + sec * WSEC + (LW + 2 * j) * 1024, vw[j], rw, s * (64 * 3456) + sec * 1152);
    };
    issue_w(s0, 0);
    if (LW == 0) { const __amdgpu_buffer_rsrc_t rg = __builtin_amdgcn_make_buffer_rsrc((void*)p.gw, 0, D * 2, 0x00020000);
        glds16(lbase + L_GW, lane * 16, rg, 0); glds16(lbase + L_GW + 1024, lane * 16, rg, 1024); }
    else if (!br) { const __amdgpu_buffer_rsrc_t rp = __builtin_amdgcn_make_buffer_rsrc((void*)p.wphi, 0, 32 * 324 * 2, 0x00020000);
#pragma unroll
        for (int i = 0; i < 3; ++i) glds16(lbase + L_WPHI + i * 1024, lane * 16, rp, g * 2592 + i * 1024); }
    issue_x(s1, BUFS); issue_w(s1, BUFS);
    unsigned nxt2 = 2 * BUFS; int k2 = s1 == 8 ? 0 : s1 + 1;
#pragma unroll 1
    for (int s = 0; s < 9; ++s) {
        if (s < 8) { if (LW) WAITV(20); else WAITV(23); } else WAITV(0);
        BAR();

        if (s + 2 < 9) { issue_x(k2, nxt2); issue_w(k2, nxt2); }
        nxt2 = (nxt2 == 2 * BUFS) ? 0 : nxt2 + BUFS; k2 = k2 == 8 ? 0 : k2 + 1;
    }
}

template <int V>
__device__ __forceinline__ void image_addr(int w, int lane, unsigned (&a0)[4], unsigned (&a1)[4]) {
    const int fg = lane >> 4, l15 = lane & 15;
#pragma unroll
    for (int n = 0; n < 4; ++n) { a0[n] = 0; a1[n] = 0; }
#pragma unroll
    for (int n = 0; n < WT<V>::NC; ++n) {
        const int T = WT<V>::T0 + n;
        if (T <= 8) {
            const int G = 4 * T + fg, q4 = (G * 57) >> 9, m = G - 9 * q4, sec = q4 >> 1, hp = q4 & 1;
            const int hstride = sec ? 384 * 48 : 128 * 48;
            const int base = (sec ? L_K + 256 * 48 : L_Q) + 2 * hp * hstride + (64 * (w & 1) + l15) * 48;
            a0[n] = base + (m < 4 ? 8 * m : (m == 4 ? 32 : hstride + 8 * (m - 5)));
            a1[n] = (m == 4) ? base + hstride + 32 : a0[n] + 4;
            asm volatile("" : "+v"(a0[n])); if (T & 1) asm volatile("" : "+v"(a1[n]));
        }
    }
    if (V >= 2) { a0[3] = L_V + l15 * VROW + (256 + 64 * (w & 1) + 4 * (((fg & 1) << 1) | (fg >> 1))) * 2; asm volatile("" : "+v"(a0[3])); }
}
template <int V>
__device__ __forceinline__ void write_images(LAS char* lds, int w, int lane, int br, const unsigned (&a0)[4], const unsigned (&a1)[4], const f32x4 (&acc)[4][4]) {
    const int fg = lane >> 4, l15 = lane & 15;
    const float qs = br ? QSCALE_B : LOG2E, ks = br ? 1.0f : LOG2E;
#pragma unroll
    for (int n = 0; n < WT<V>::NC; ++n) {
        const int T = WT<V>::T0 + n;
        if (T <= 8) {
            const float sc = T < 4 ? qs : (T > 4 ? ks : (fg < 2 ? qs : ks));
#pragma unroll
            for (int tt = 0; tt < 4; ++tt) {
                const unsigned lo = pack2(acc[tt][n][0] * sc, acc[tt][n][1] * sc), hi = pack2(acc[tt][n][2] * sc, acc[tt][n][3] * sc);
                if (T & 1) { *(LAS unsigned*)(lds + a0[n] + tt * 768) = lo; *(LAS unsigned*)(lds + a1[n] + tt * 768) = hi; }
                else *(LAS u32x2*)(lds + a0[n] + tt * 768) = u32x2{lo, hi};
            }
        } else if (T < 13 || l15 < 8) {
#pragma unroll
            for (int tt = 0; tt < 4; ++tt)
                *(LAS u32x2*)(lds + a0[3] + (T - 9) * 16 * VROW + tt * 32) = u32x2{pack2(acc[tt][n][0], acc[tt][n][1]), pack2(acc[tt][n][2], acc[tt][n][3])};
        }
    }
    if (V == 3 && l15 == 8) {
#pragma unroll
        for (int tt = 0; tt < 4; ++tt) *(LAS f32x4*)(lds + L_G + (64 * (w & 1) + 16 * tt + 4 * fg) * 4) = acc[tt][2];
    }
}
__device__ __forceinline__ float gate_at(const LAS char* lds, int tok) {
    return __builtin_amdgcn_rcpf(1.0f + __builtin_amdgcn_exp2f(-LOG2E * *(const LAS float*)(lds + L_G + tok * 4)));
}

__device__ __forceinline__ void write_o(LAS char* ob, int lane, int head, int tok0, const f32x16& o, float scale) {
    const int h = lane >> 5, tok = tok0 + (lane & 31);
    LAS char* a = ob + tok * 144 + head * 36 + 8 * h;
    *(LAS unsigned*)(a) = pack2(o[0] * scale, o[1] * scale);
    *(LAS unsigned*)(a + 4) = pack2(o[2] * scale, o[3] * scale);
    *(LAS unsigned*)(a + 16) = pack2(o[4] * scale, o[5] * scale);
    *(LAS unsigned*)(a + 20) = pack2(o[6] * scale, o[7] * scale);
    if (h == 0) *(LAS unsigned*)(a + 32) = pack2(o[8] * scale, o[9] * scale);
}

enum { M_NONE, M_ZERO, M_DIAG, M_TAIL };
struct AttnState { f32x16 oA, oB, sA, sB, mD; bf16x8 qA0, qB0; bf16x4 qA1, qB1; const LAS char* kp; const LAS char* k1p; const LAS char* vp; };
__device__ __forceinline__ void exp_pack(const f32x16& s, bf16x8& p0, bf16x8& p1) {
    u32x4 a, b;
#pragma unroll
    for (int i = 0; i < 4; ++i) {
        a[i] = pack2(__builtin_amdgcn_exp2f(s[2 * i]), __builtin_amdgcn_exp2f(s[2 * i + 1]));
        b[i] = pack2(__builtin_amdgcn_exp2f(s[8 + 2 * i]), __builtin_amdgcn_exp2f(s[9 + 2 * i]));
    }
    p0 = __builtin_bit_cast(bf16x8, a); p1 = __builtin_bit_cast(bf16x8, b);
}
template <bool TAIL>
__device__ __forceinline__ f32x16 make_mask(float rbig) {
    f32x16 m;
#pragma unroll
    for (int i = 0; i < 16; ++i) { const float t = rbig - (float)((i & 3) + 8 * (i >> 2)) * 1.2676506002282294e30f; m[i] = __builtin_amdgcn_fmed3f(TAIL ? -t : t, 0.f, -3.0e38f); }
    return m;
}
__device__ __forceinline__ f32x16 attn_slow(const LAS char* kp, const LAS char* vp, bf16x8 q0, bf16x4 q1, int kb0, int kbd, bool tail, int lane, float rbig) {
    const int h = lane >> 5;
    float m = -3.0e38f;
    f32x16 o = {};
#pragma unroll 1
    for (int pass = 0; pass < 2; ++pass) {
#pragma unroll 1
        for (int kb = kb0; kb <= kbd; ++kb) {
            const bf16x8 k0 = *(const LAS bf16x8*)(kp + kb * 1536);
            const bf16x4 k1 = __builtin_bit_cast(bf16x4, u32x2{*(const LAS unsigned*)(kp + kb * 1536 + 32 - 16 * h), 0u});
            f32x16 c = {};
            if (kb == kbd) c = make_mask<false>(rbig); else if (tail && kb == kb0) c = make_mask<true>(rbig);
            f32x16 sc = mfma32k8(k1, q1, mfma32(k0, q0, c));
            if (pass == 0) {
#pragma unroll
                for (int i = 0; i < 16; ++i) m = fmaxf(m, sc[i]);
            } else {
#pragma unroll
                for (int i = 0; i < 16; ++i) sc[i] -= m;
                bf16x8 p0, p1; exp_pack(sc, p0, p1);
                const bf16x8 v0 = *(const LAS bf16x8*)(vp + kb * 64), v1 = *(const LAS bf16x8*)(vp + kb * 64 + 32);
                o = mfma32(v0, p0, o); o = mfma32(v1, p1, o);
            }
        }
        if (pass == 0) m = swap_max(m);
    }
    return o;
}
template <int KB, int NA, int NB>
__device__ __forceinline__ void attn_scores(const AttnState& st, f32x16& nA, f32x16& nB) {
    const bf16x8 k0 = *(const LAS bf16x8*)(st.kp + KB * 1536);
    const bf16x4 k1 = __builtin_bit_cast(bf16x4, u32x2{*(const LAS unsigned*)(st.k1p + KB * 1536), 0u});
    if (NA != M_NONE) nA = mfma32k8(k1, st.qA1, mfma32(k0, st.qA0, NA == M_ZERO ? f32x16{} : st.mD));
    if (NB != M_NONE) nB = mfma32k8(k1, st.qB1, mfma32(k0, st.qB0, NB == M_ZERO ? f32x16{} : st.mD));
}
template <int KB, int NA, int NB>
__device__ __forceinline__ void attn_first(AttnState& st) { attn_scores<KB, NA, NB>(st, st.sA, st.sB); __builtin_amdgcn_sched_barrier(0); }
template <int KB, bool A, bool B, int NA, int NB>
__device__ __forceinline__ void attn_step(AttnState& st) {
    const bf16x8 v0 = *(const LAS bf16x8*)(st.vp + KB * 64), v1 = *(const LAS bf16x8*)(st.vp + KB * 64 + 32);
    f32x16 nA, nB;
    if (NA != M_NONE || NB != M_NONE) attn_scores<(KB > 0 ? KB - 1 : 0), NA, NB>(st, nA, nB);
    bf16x8 p0, p1;
    if (A) { exp_pack(st.sA, p0, p1); st.oA = mfma32(v0, p0, st.oA); st.oA = mfma32(v1, p1, st.oA); }
    if (B) { exp_pack(st.sB, p0, p1); st.oB = mfma32(v0, p0, st.oB); st.oB = mfma32(v1, p1, st.oB); }
    if (NA != M_NONE) st.sA = nA;
    if (NB != M_NONE) st.sB = nB;
    __builtin_amdgcn_sched_group_barrier(0x100, 4, 0);
#define SGA __builtin_amdgcn_sched_group_barrier(0x400, 4, 0); __builtin_amdgcn_sched_group_barrier(0x002, 2, 0); __builtin_amdgcn_sched_group_barrier(0x008, 1, 0);
    SGA SGA SGA SGA SGA SGA SGA SGA
#undef SGA
    __builtin_amdgcn_sched_barrier(0);
}

__device__ __forceinline__ void halo_first(const Params& p, LAS char* lds, int tid, int rb, int g) {
    const __amdgpu_buffer_rsrc_t rws = __builtin_amdgcn_make_buffer_rsrc((void*)p.ws, 0, 16 << 20, 0x00020000);
    const unsigned src = (unsigned)WS_KG + ((rb >= 1 ? rb - 1 : 0) * 8 + g) * KG_BYTES;
    u32x4 v[5]; unsigned la[5];
#pragma unroll
    for (int j = 0; j < 5; ++j) { const int c = tid + 640 * j; const bool ok = (j < 4 || c < 2688) && rb >= 1;
        v[j] = __builtin_amdgcn_raw_buffer_load_b128(rws, ok ? src + c * 16 : 0xFFFFFF00u, 0, 16);
        int a;
        if (j < 2 || (j == 2 && c < 1536)) { const int hd = (c * 683) >> 18, wi = c - 384 * hd; a = L_K + hd * 18432 + 6144 + wi * 16; }
        else { const int cc = c - 1536; a = L_V + (cc >> 4) * VROW + 256 + (cc & 15) * 16; }
        la[j] = ok ? a : L_G + 1024 + (tid & 15) * 16; }
    __builtin_amdgcn_sched_barrier(0);
#pragma unroll
    for (int j = 0; j < 5; ++j) *(LAS u32x4*)(lds + la[j]) = v[j];
}
#define KV_ARRIVE() do { WAITV(0); unsigned prev = 0; \
            if (lane == 0) prev = __hip_atomic_fetch_add((LAS unsigned*)(lds + L_G + 512), 1u, __ATOMIC_RELAXED, __HIP_MEMORY_SCOPE_WORKGROUP); \
            if (lane == 0 && prev == 5) set_flag(p.ws + WS_FKV + (rb * 8 + g) * 128, tag); } while (0)
template <int J0>
__device__ __forceinline__ void attn_wave(const Params& p, LAS char* lds, int w, int lane, int rb, int g, u64 tag) {
    const int head = w & 3, h = lane >> 5, r31 = lane & 31;
    AttnState st;
    { const LAS char* qp = lds + L_Q + head * 6144 + (32 * J0 + r31) * 48;
      st.qA0 = *(const LAS bf16x8*)(qp + 16 * h); st.qB0 = *(const LAS bf16x8*)(qp + 64 * 48 + 16 * h);
      unsigned ta = *(const LAS unsigned*)(qp + 32), tb = *(const LAS unsigned*)(qp + 64 * 48 + 32);
      st.qA1 = __builtin_bit_cast(bf16x4, u32x2{h ? 0u : ta, 0u}); st.qB1 = __builtin_bit_cast(bf16x4, u32x2{h ? 0u : tb, 0u}); }
    float rbig = ((float)(r31 - 4 * h) + 0.5f) * 1.2676506002282294e30f;
    st.mD = make_mask<false>(rbig);
    st.oA = f32x16{}; st.oB = f32x16{};
    st.kp = lds + L_K + head * 18432 + r31 * 48 + 16 * h;
    st.k1p = lds + L_K + head * 18432 + r31 * 48 + 32;
    st.vp = lds + L_V + (r31 < 18 ? head * 18 + r31 : 72) * VROW + 16 * h;
    attn_first<J0 + 10, M_NONE, M_DIAG>(st);
    if (J0 == 0) KV_ARRIVE();
    attn_step<J0 + 10, false, true, M_NONE, M_ZERO>(st);
    attn_step<J0 + 9, false, true, M_DIAG, M_ZERO>(st);
    if (J0) { attn_step<9, true, true, M_ZERO, M_ZERO>(st); attn_step<8, true, true, M_NONE, M_NONE>(st); }
    else attn_step<8, true, true, M_NONE, M_NONE>(st);

    BAR();
    halo_first(p, lds, (w << 6) + lane, rb, g);
    BAR();

    if (rb >= 1) {
        attn_first<7, M_ZERO, M_ZERO>(st);
        attn_step<7, true, true, M_ZERO, M_ZERO>(st);
        attn_step<6, true, true, M_ZERO, M_ZERO>(st);
        attn_step<5, true, true, M_ZERO, M_ZERO>(st);
        attn_step<4, true, true, M_NONE, M_NONE>(st);
    }
    BAR();

    if (rb >= 2) {
        asm volatile("" : "+v"(rbig));
        st.mD = make_mask<true>(rbig);
        if (J0 == 0) { attn_first<3, M_ZERO, M_ZERO>(st); attn_step<3, true, true, M_ZERO, M_TAIL>(st); }
        else attn_first<3, M_ZERO, M_TAIL>(st);
        attn_step<J0 + 2, true, true, M_ZERO, M_NONE>(st);
        attn_step<J0 + 1, true, false, M_TAIL, M_NONE>(st);
        attn_step<J0, true, false, M_NONE, M_NONE>(st);
    }
    float lA = swap_lo(st.oA[10]), lB = swap_lo(st.oB[10]);
    if (__builtin_expect(__any(!(lA >= 9.3e-10f && lA <= 1.07e9f && lB >= 9.3e-10f && lB <= 1.07e9f)) || FORCE_FB, 0)) {
#pragma unroll 1
        for (int it = 0; it < 2; ++it) {
            const int j = J0 + 2 * it, J = 4 * rb + j, nb = J < 8 ? J : 8;
            const f32x16 o = attn_slow(st.kp, st.vp, it ? st.qB0 : st.qA0, it ? st.qB1 : st.qA1, 8 + j - nb, 8 + j, J >= 8, lane, rbig);
            if (it) st.oB = o; else st.oA = o;
        }
        lA = swap_lo(st.oA[10]); lB = swap_lo(st.oB[10]);
    }
    const float gA = gate_at(lds, 32 * J0 + r31), gB = gate_at(lds, 32 * J0 + 64 + r31);
    write_o(lds + L_Q, lane, head, 32 * J0, st.oA, (1.0f - gA) * __builtin_amdgcn_rcpf(lA));
    write_o(lds + L_Q, lane, head, 32 * J0 + 64, st.oB, (1.0f - gB) * __builtin_amdgcn_rcpf(lB));
}

__device__ __forceinline__ void features2(const LAS char* rowp, int stride, int lane, bf16x8 wf0, bf16x4 wf1, unsigned (&pk)[2][9]) {
    const int h = lane >> 5;
    f32x16 d[2];
#pragma unroll
    for (int b = 0; b < 2; ++b) {
        const bf16x8 b0 = *(const LAS bf16x8*)(rowp + b * stride + 16 * h);
        const unsigned t = *(const LAS unsigned*)(rowp + b * stride + 32);
        d[b] = mfma32k8(wf1, __builtin_bit_cast(bf16x4, u32x2{h ? 0u : t, 0u}), mfma32(wf0, b0, f32x16{}));
    }
    float ep[2][9], en[2][9], sp[2], sn[2];
#pragma unroll
    for (int i = 0; i < 9; ++i)
#pragma unroll
        for (int b = 0; b < 2; ++b) { ep[b][i] = __builtin_amdgcn_exp2f(d[b][i]); en[b][i] = __builtin_amdgcn_exp2f(-d[b][i]); }
#define SUM9(e) (((e[0] + e[1]) + (e[2] + e[3])) + ((e[4] + e[5]) + (e[6] + e[7])) + e[8])
#pragma unroll
    for (int b = 0; b < 2; ++b) { sp[b] = SUM9(ep[b]); sn[b] = SUM9(en[b]); }
#pragma unroll
    for (int b = 0; b < 2; ++b) { sp[b] = swap_sum(sp[b]); sn[b] = swap_sum(sn[b]); }
    constexpr float BIG = 1.152921504606847e18f;
    if (__builtin_expect(__any(!(sp[0] < BIG && sn[0] < BIG && sp[1] < BIG && sn[1] < BIG)) || FORCE_FB, 0)) {
#pragma unroll
        for (int b = 0; b < 2; ++b) {
            float mx = d[b][0], mn = d[b][0];
#pragma unroll
            for (int i = 1; i < 9; ++i) { mx = fmaxf(mx, d[b][i]); mn = fminf(mn, d[b][i]); }
            mx = swap_max(mx); mn = swap_min(mn);
#pragma unroll
            for (int i = 0; i < 9; ++i) { ep[b][i] = __builtin_amdgcn_exp2f(d[b][i] - mx); en[b][i] = __builtin_amdgcn_exp2f(mn - d[b][i]); }
            sp[b] = swap_sum(SUM9(ep[b])); sn[b] = swap_sum(SUM9(en[b]));
        }
    }
#undef SUM9
#pragma unroll
    for (int b = 0; b < 2; ++b) { sp[b] = __builtin_amdgcn_rcpf(sp[b]); sn[b] = __builtin_amdgcn_rcpf(sn[b]); }
#pragma unroll
    for (int b = 0; b < 2; ++b) {
        float v[18];
#pragma unroll
        for (int i = 0; i < 9; ++i) { v[i] = ep[b][i] * sp[b]; v[9 + i] = en[b][i] * sn[b]; }
#pragma unroll
        for (int i = 0; i < 9; ++i) pk[b][i] = pack2(v[2 * i], v[2 * i + 1]);
    }
}

constexpr int P3_R = 76, P3_GC = P3_R * 9, P3_GB = 11008, P3_OQ = 4 * P3_GB,
              P3_W0 = 2 * P3_OQ, P3_WQ = 36864, P3_END = P3_W0 + 2 * P3_WQ;
constexpr int L_RDY = P3_END;
static_assert(P3_END + 256 <= LDS_BYTES, "phase 3 LDS map");
__device__ __forceinline__ void lds_wait(LAS char* lds, int off) {
    for (unsigned spins = 0; spins < (1u << 22); ++spins) {
        if (__builtin_amdgcn_readfirstlane(__hip_atomic_load((LAS unsigned*)(lds + off), __ATOMIC_RELAXED, __HIP_MEMORY_SCOPE_WORKGROUP))) break;
        __builtin_amdgcn_s_sleep(1);
    }
    asm volatile("" ::: "memory");
}
__device__ __forceinline__ void phase3(const Params& p, LAS char* lds, int tid, int lane, int w, int t0, int ct, u64 tag) {
    if (w == 9) {
        const int rb0 = t0 >> 7, rb1 = (t0 + P3_R - 1 < S ? t0 + P3_R - 1 : S - 1) >> 7;
        const char* fl = p.ws + WS_FO + (((lane & 8) ? rb1 : rb0) * 16 + (lane & 7)) * 128;
        wait_flags(fl, lane < 16, tag, p.ws);
        if (lane == 0) __hip_atomic_store((LAS unsigned*)(lds + L_RDY), 1u, __ATOMIC_RELAXED, __HIP_MEMORY_SCOPE_WORKGROUP);
        wait_flags(fl + 8 * 128, lane < 16, tag, p.ws);
        if (lane == 0) __hip_atomic_store((LAS unsigned*)(lds + L_RDY + 4), 1u, __ATOMIC_RELAXED, __HIP_MEMORY_SCOPE_WORKGROUP);

        asm volatile("s_waitcnt lgkmcnt(0) vmcnt(0)" ::: "memory");
        __builtin_amdgcn_endpgm();
    } else if (w == 8) {
        const unsigned lbase = (unsigned)(size_t)lds;
        const __amdgpu_buffer_rsrc_t rwa = __builtin_amdgcn_make_buffer_rsrc((void*)p.woA, 0, D * D * 2, 0x00020000);
        const __amdgpu_buffer_rsrc_t rwb = __builtin_amdgcn_make_buffer_rsrc((void*)p.woB, 0, D * D * 2, 0x00020000);
        const int wrow = lane >> 3;
        const unsigned vwo0 = wrow * 1152 + ct * 128 + (((lane & 7) ^ (((wrow >> 1) & 1) << 1)) << 4), vwo1 = vwo0 ^ 64;
        auto issue_w = [&](int q) {
            const unsigned lb = lbase + P3_W0 + (q & 1) * P3_WQ, so = (q & 1) * 288 * 1152;
#pragma unroll 2
            for (int u = 0; u < 36; ++u) glds16(lb + u * 1024, (u & 1) ? vwo1 : vwo0, q < 2 ? rwa : rwb, so + u * 8 * 1152);
        };
        __builtin_amdgcn_s_setprio(3);
        issue_w(0); issue_w(1); WAITV(36); BAR();
        WAITV(0); BAR();
        issue_w(2); WAITV(0); BAR();
        issue_w(3); WAITV(0); BAR();
    } else if (w >= 4) {
        const __amdgpu_buffer_rsrc_t ro = __builtin_amdgcn_make_buffer_rsrc((void*)(p.ws + WS_O), 0, 16 * 2048 * 144, 0x00020000);
        const int t4 = tid - 256;
        unsigned oo[11], ol[11];
#pragma unroll
        for (int j = 0; j < 11; ++j) { const int c = t4 + 256 * j, sl = (c * 6133) >> 22, wi = c - P3_GC * sl;
            oo[j] = c < 4 * P3_GC ? (sl * 2048 + t0) * 144 + wi * 16 : 0xC0000000u; ol[j] = c < 4 * P3_GC ? sl * P3_GB + wi * 16 : P3_END + 128 + (lane & 7) * 16; }
        u32x4 x[11], y[11];
        auto load_o = [&](u32x4 (&r)[11], int q) {
#pragma unroll
            for (int j = 0; j < 11; ++j) r[j] = __builtin_amdgcn_raw_buffer_load_b128(ro, oo[j], q * (4 * 2048 * 144), 16);
            __builtin_amdgcn_sched_barrier(0);
        };
        auto write_ob = [&](const u32x4 (&r)[11], int buf) {
#pragma unroll
            for (int j = 0; j < 10; ++j) *(LAS u32x4*)(lds + buf * P3_OQ + ol[j]) = r[j];
            *(LAS u32x4*)(lds + (t4 + 2560 < 4 * P3_GC ? buf * P3_OQ : 0) + ol[10]) = r[10];
            __builtin_amdgcn_sched_barrier(0);
        };
        lds_wait(lds, L_RDY);
        load_o(x, 0); load_o(y, 1);
        write_ob(x, 0);
        BAR();
        lds_wait(lds, L_RDY + 4);
        load_o(x, 2);
        write_ob(y, 1);
        load_o(y, 3);
        BAR();
        write_ob(x, 0);
        BAR();
        write_ob(y, 1);
        BAR();
    } else {
        const int fg = lane >> 4, l15 = lane & 15;
        const int fl2 = ((l15 >> 3) & 1) | ((fg & 1) << 1);
        const unsigned wl0 = P3_W0 + (72 * fg + (l15 >> 2)) * 128 + ((w ^ fl2) << 5) + 8 * (lane & 3);
        const unsigned ofb = fg * P3_GB + l15 * 144;
        f32x4 acc[5];
#pragma unroll
        for (int a = 0; a < 5; ++a) acc[a] = f32x4{0, 0, 0, 0};
        struct F3 { bf16x8 w, o[5]; };
        auto compute = [&](int qodd) {
            const unsigned we = wl0 + qodd * P3_WQ, wo_ = we ^ 64, ob = ofb + qodd * P3_OQ;
            auto rd = [&](F3& f, int j) { const unsigned wa = ((j & 1) ? wo_ : we) + j * 1024;
                f.w = cat(trread(lds + wa), trread(lds + wa + 512));
#pragma unroll
                for (int a = 0; a < 5; ++a) f.o[a] = *(const LAS bf16x8*)(lds + ob + j * 16 + a * 16 * 144); };
            auto mm = [&](const F3& f) {
#pragma unroll
                for (int a = 0; a < 5; ++a) acc[a] = mfma16(f.w, f.o[a], acc[a]); };
            F3 f0, f1, f2;
            rd(f0, 0); rd(f1, 1);
            __builtin_amdgcn_sched_barrier(0);
#define P3STEP(FA, FC, j) do { if ((j) + 2 < 9) rd(FC, (j) + 2); mm(FA); \
            __builtin_amdgcn_sched_group_barrier(0x100, 2, 0); __builtin_amdgcn_sched_group_barrier(0x008, 1, 0); __builtin_amdgcn_sched_group_barrier(0x100, 2, 0); __builtin_amdgcn_sched_group_barrier(0x008, 1, 0); \
            __builtin_amdgcn_sched_group_barrier(0x100, 1, 0); __builtin_amdgcn_sched_group_barrier(0x008, 1, 0); __builtin_amdgcn_sched_group_barrier(0x100, 2, 0); __builtin_amdgcn_sched_group_barrier(0x008, 2, 0); \
            __builtin_amdgcn_sched_barrier(0); } while (0)
            P3STEP(f0, f2, 0); P3STEP(f1, f0, 1); P3STEP(f2, f1, 2); P3STEP(f0, f2, 3); P3STEP(f1, f0, 4); P3STEP(f2, f1, 5); P3STEP(f0, f2, 6); P3STEP(f1, f0, 7); P3STEP(f2, f1, 8);
#undef P3STEP
        };
#pragma unroll 1
        for (int hf = 0; hf < 2; ++hf) {
            BAR();
            compute(0);
            BAR();
            compute(1);
        }
        const __amdgpu_buffer_rsrc_t rout = __builtin_amdgcn_make_buffer_rsrc(p.out, 0, S * D * 2, 0x00020000);
#pragma unroll
        for (int a = 0; a < 5; ++a) { const int tr = 16 * a + l15, trow = t0 + tr;
            __builtin_amdgcn_raw_buffer_store_b64(u32x2{pack2(acc[a][0], acc[a][1]), pack2(acc[a][2], acc[a][3])}, rout,
                                                  (tr < P3_R && trow < S) ? trow * 1152 + ct * 128 + w * 32 + fg * 8 : 0xFFFFFF00u, 0, 0); }
    }
}

__global__ __launch_bounds__(640, 3) void k_fused(Params p) {
    extern __shared__ __attribute__((aligned(16))) char smem[];
    LAS char* lds = (LAS char*)smem;
    const int tid = threadIdx.x, lane = tid & 63, w = __builtin_amdgcn_readfirstlane(tid >> 6);
    const int bid = blockIdx.x, g = bid & 7, br = bid < 128, rb = (bid >> 3) & 15, r0 = rb * 128;
    const int s0 = (rb * 5 + g * 2 + br * 4) % 9;
    {
        u64 pc; unsigned len;
        asm volatile("s_getpc_b64 %0\n.Lpf_here%=:\n\ts_mov_b32 %1, k_fused_code_end-.Lpf_here%=" : "=s"(pc), "=s"(len));
        const __amdgpu_buffer_rsrc_t rc = __builtin_amdgcn_make_buffer_rsrc((void*)pc, 0, len, 0x00020000);
        asm volatile("s_mov_b32 m0, %0\n\ts_nop 0\n\tbuffer_load_dword %1, %2, 0 offen lds" :: "s"((unsigned)(size_t)lds + L_G + 1024), "v"((unsigned)threadIdx.x * 128u), "s"(rc) : "memory");
    }

    const __amdgpu_buffer_rsrc_t rws = __builtin_amdgcn_make_buffer_rsrc((void*)p.ws, 0, 16 << 20, 0x00020000);
    if (__builtin_expect(w >= 8, 1)) {
        if (w == 8 && lane == 0) *(LAS unsigned*)(lds + L_G + 512) = 0u;
        if (w == 8) load_loop<0>(p, lds, lane, g, br, r0, s0); else load_loop<1>(p, lds, lane, g, br, r0, s0);

        BAR();
        if (w == 8 && lane < 49) *(LAS u32x4*)(lds + L_V + 72 * VROW + lane * 16) = u32x4{0x3F803F80u, 0x3F803F80u, 0x3F803F80u, 0x3F803F80u};
        if (!br) {
            const int r31 = lane & 31, h = lane >> 5, hh = (r31 >> 2) & 1, i = (r31 & 3) + 4 * (r31 >> 3); const bool valid = i <= 8 && r31 < 24; const int f = valid ? 9 * hh + i : 0;
            const unsigned m = valid ? 0xFFFFFFFFu : 0u;
            unsigned v[2][10];
#pragma unroll
            for (int e = 0; e < 2; ++e) { const LAS unsigned short* wp = (const LAS unsigned short*)(lds + L_WPHI) + (2 * (w - 8) + e) * 324 + f;
#pragma unroll
                for (int j = 0; j < 8; ++j) v[e][j] = wp[(8 * h + j + 2 * e) * 18];
                v[e][8] = wp[(e ? 0 : 16) * 18]; v[e][9] = wp[(e ? 1 : 17) * 18]; }
#pragma unroll
            for (int e = 0; e < 2; ++e) { const int hd = 2 * (w - 8) + e;
                *(LAS u32x4*)(lds + L_V + (20 + 2 * hd + h) * VROW + r31 * 16) = u32x4{(v[e][0] | v[e][1] << 16) & m, (v[e][2] | v[e][3] << 16) & m, (v[e][4] | v[e][5] << 16) & m, (v[e][6] | v[e][7] << 16) & m};
                *(LAS u32x2*)(lds + L_V + (28 + hd) * VROW + lane * 8) = u32x2{h ? 0u : (v[e][8] | v[e][9] << 16) & m, 0u}; }
        }
    } else {
        if (tid == 0) *(LAS u64*)(lds + L_G + 520) = __hip_atomic_load((const GAS u64*)(p.ws + WS_EPOCH), __ATOMIC_RELAXED, __HIP_MEMORY_SCOPE_AGENT) + 1;
        f32x4 acc[4][4];
#pragma unroll
        for (int t = 0; t < 4; ++t)
#pragma unroll
            for (int n = 0; n < 4; ++n) acc[t][n] = f32x4{0, 0, 0, 0};
        unsigned a0[4], a1[4];
#define RUN(V) do { image_addr<V>(w, lane, a0, a1); gemm_loop<V>(lds, w, lane, s0, acc); __builtin_amdgcn_s_setprio(0); BAR(); write_images<V>(lds, w, lane, br, a0, a1, acc); } while (0)
        const int v = ((w >> 1) & 1) * 2 + (w >> 2);
        if (w >= 4) __builtin_amdgcn_s_setprio(1);
        if (v == 0) RUN(0); else if (v == 1) RUN(1); else if (v == 2) RUN(2); else RUN(3);
#undef RUN
    }
    BAR();
    u64 tag;
    { const u32x2 t2 = *(const LAS u32x2*)(lds + L_G + 520); tag = ((u64)(unsigned)__builtin_amdgcn_readfirstlane(t2[1]) << 32) | (unsigned)__builtin_amdgcn_readfirstlane(t2[0]); }

    const int head = w & 3, h = lane >> 5, r31 = lane & 31;
    LAS char* ob = lds + L_Q;
    if (__builtin_expect(br, 1)) {
        if (rb < 15 && (w < 4 || w >= 8)) {
            const unsigned kg = (unsigned)WS_KG + (rb * 8 + g) * KG_BYTES; const int t6 = (w < 4 ? w : w - 4) * 64 + lane;
            u32x4 pv[7];
#pragma unroll
            for (int j = 0; j < 4; ++j) pv[j] = *(const LAS u32x4*)(lds + L_K + j * 18432 + 12288 + t6 * 16);
#pragma unroll
            for (int j = 0; j < 3; ++j) { const int cc = t6 + 384 * j; pv[4 + j] = *(const LAS u32x4*)(lds + L_V + (cc >> 4) * VROW + 512 + (cc & 15) * 16); }
#pragma unroll
            for (int j = 0; j < 7; ++j) __builtin_amdgcn_raw_buffer_store_b128(pv[j], rws, kg + (t6 + 384 * j) * 16, 0, 16);
        }
        if (__builtin_expect(w >= 8, 0)) {
            const int t2 = tid - 512;
            KV_ARRIVE();
            if (rb > 0) wait_flags(p.ws + WS_FKV + ((rb - 1 - (lane & 1)) * 8 + g) * 128, lane < 2 && rb - 1 - lane >= 0, tag, p.ws);
            BAR();
            halo_first(p, lds, tid, rb, g);
            BAR();
            u32x4 v[21];
            const unsigned src = (unsigned)WS_KG + ((rb >= 2 ? rb - 2 : 0) * 8 + g) * KG_BYTES + t2 * 16;
#pragma unroll
            for (int j = 0; j < 21; ++j) v[j] = __builtin_amdgcn_raw_buffer_load_b128(rws, rb >= 2 ? src + j * 2048 : 0xFFFFFF00u, 0, 16);
            __builtin_amdgcn_sched_barrier(0);
#pragma unroll
            for (int j = 0; j < 12; ++j) { const int c = t2 + 128 * j, hd = j / 3, wi = c - 384 * hd;
                *(LAS u32x4*)(lds + L_K + hd * 18432 + wi * 16) = v[j]; }
#pragma unroll
            for (int j = 0; j < 9; ++j) { const int c = t2 + 128 * j;
                *(LAS u32x4*)(lds + L_V + (c >> 4) * VROW + (c & 15) * 16) = v[12 + j]; }
            BAR();
        } else {
            if (w >= 4) __builtin_amdgcn_s_setprio(1);
            if (w < 4) attn_wave<0>(p, lds, w, lane, rb, g, tag); else attn_wave<1>(p, lds, w, lane, rb, g, tag);
            __builtin_amdgcn_s_setprio(0);
        }
    } else {
        const int fg = lane >> 4, l15 = lane & 15, th = (w >> 2) & 1;
        LAS char* kvb = lds + L_V;
        LAS char* pt = lds + L_K + head * 18432;
        unsigned pq[2][9];
        f32x4 kv[3][2];
        bf16x8 wf0; bf16x4 wf1;
        if (w < 8) {
            if (w >= 4) __builtin_amdgcn_s_setprio(1);
            wf0 = *(const LAS bf16x8*)(lds + L_V + (20 + 2 * head + h) * VROW + r31 * 16); wf1 = *(const LAS bf16x4*)(lds + L_V + (28 + head) * VROW + lane * 8);
            unsigned pk[2][9];
            features2(lds + L_K + head * 18432 + (256 + 64 * th + r31) * 48, 32 * 48, lane, wf0, wf1, pk);
            const LAS char* vb0p = lds + L_V + (head * 18 + l15) * VROW + (256 + 64 * th + 8 * fg) * 2;
            const LAS char* vb1p = lds + L_V + (l15 < 2 ? head * 18 + 16 + l15 : 72) * VROW + (256 + 64 * th + 8 * fg) * 2;
            bf16x8 vb0[2], vb1[2];
#pragma unroll
            for (int tb = 0; tb < 2; ++tb) { vb0[tb] = *(const LAS bf16x8*)(vb0p + tb * 64); vb1[tb] = *(const LAS bf16x8*)(vb1p + tb * 64); }
#pragma unroll
            for (int tb = 0; tb < 2; ++tb) {
                LAS char* wr = pt + (2 * th + tb) * 2560 + r31 * 80 + 40 * h;
                *(LAS u32x2*)(wr) = u32x2{pk[tb][0], pk[tb][1]}; *(LAS u32x2*)(wr + 8) = u32x2{pk[tb][2], pk[tb][3]}; *(LAS u32x2*)(wr + 16) = u32x2{pk[tb][4], pk[tb][5]};
                *(LAS u32x2*)(wr + 24) = u32x2{pk[tb][6], pk[tb][7]}; *(LAS unsigned*)(wr + 32) = pk[tb][8];
            }
            const LAS char* tr = pt + 2 * th * 2560 + (16 * (fg >> 1) + 4 * (fg & 1) + (l15 >> 2)) * 80 + 8 * (lane & 3);
            bf16x8 af[2][3];
#pragma unroll
            for (int tb = 0; tb < 2; ++tb)
#pragma unroll
                for (int ft = 0; ft < 3; ++ft) af[tb][ft] = cat(trread(tr + tb * 2560 + ft * 32), trread(tr + tb * 2560 + ft * 32 + 8 * 80));
#pragma unroll
            for (int ft = 0; ft < 3; ++ft) {
                kv[ft][0] = mfma16(af[1][ft], vb0[1], mfma16(af[0][ft], vb0[0], f32x4{0, 0, 0, 0}));
                kv[ft][1] = mfma16(af[1][ft], vb1[1], mfma16(af[0][ft], vb1[0], f32x4{0, 0, 0, 0}));
            }
            if (th == 1) {
#pragma unroll
                for (int ft = 0; ft < 3; ++ft) { *(LAS f32x4*)(pt + 5120 + (2 * ft) * 1024 + lane * 16) = kv[ft][0]; *(LAS f32x4*)(pt + 5120 + (2 * ft + 1) * 1024 + lane * 16) = kv[ft][1]; }
            }
            __builtin_amdgcn_s_setprio(0);
        }
        BAR();

        if (w < 4) {
            f32x4 u[3][2];
#pragma unroll
            for (int ft = 0; ft < 3; ++ft) { u[ft][0] = *(const LAS f32x4*)(pt + 5120 + (2 * ft) * 1024 + lane * 16); u[ft][1] = *(const LAS f32x4*)(pt + 5120 + (2 * ft + 1) * 1024 + lane * 16); }
            LAS char* kr = pt;
#pragma unroll
            for (int ft = 0; ft < 3; ++ft)
#pragma unroll
                for (int dt = 0; dt < 2; ++dt) { const int drow = 16 * dt + l15, f = 16 * ft + 4 * fg; const f32x4 t = kv[ft][dt] + u[ft][dt];
                    *(LAS u32x2*)(kr + ((drow < 19 && f < 40) ? drow * 80 + f * 2 : 2048 + lane * 8)) = u32x2{pack2(t[0], t[1]), pack2(t[2], t[3])}; }
            const unsigned kvp = (unsigned)WS_KVP + (g * 16 + rb) * KVP_BYTES + head * 1520;
#pragma unroll
            for (int j = 0; j < 2; ++j) { const int c = lane + 64 * j;
                __builtin_amdgcn_raw_buffer_store_b128(*(const LAS u32x4*)(kr + c * 16), rws, c < 95 ? kvp + c * 16 : 0xFFFFFF00u, 0, 16); }
        }
        float gt[2] = {0.f, 0.f};
        if (w < 8) { gt[0] = gate_at(lds, 64 * th + r31); gt[1] = gate_at(lds, 64 * th + 32 + r31); }
        if (w < 8) features2(lds + L_Q + head * 6144 + (64 * th + r31) * 48, 32 * 48, lane, wf0, wf1, pq);
        if (w < 4) {
            WAITV(0);
            unsigned prev = 0;
            if (lane == 0) prev = __hip_atomic_fetch_add((LAS unsigned*)(lds + L_G + 512), 1u, __ATOMIC_RELAXED, __HIP_MEMORY_SCOPE_WORKGROUP);
            if (lane == 0 && prev == 3) set_flag(p.ws + WS_FKP + (rb * 8 + g) * 128, tag);
        }

        if (w == 8) wait_flags(p.ws + WS_FKP + ((lane & 15) * 8 + g) * 128, lane < 16, tag, p.ws);
        BAR();

        if (tid < KVP_CHUNKS) {
            u32x4 v[16];
#pragma unroll
            for (int i = 0; i < 16; ++i) v[i] = __builtin_amdgcn_raw_buffer_load_b128(rws, (unsigned)WS_KVP + (g * 16 + i) * KVP_BYTES + tid * 16, 0, 16);
            f32x2 sum2[4];
#pragma unroll
            for (int e = 0; e < 4; ++e) sum2[e] = f32x2{0.f, 0.f};
#pragma unroll
            for (int i = 0; i < 16; ++i)
#pragma unroll
                for (int e = 0; e < 4; ++e) sum2[e] += f32x2{__uint_as_float(v[i][e] << 16), __uint_as_float(v[i][e] & 0xffff0000u)};
            const float sum[8] = {sum2[0][0], sum2[0][1], sum2[1][0], sum2[1][1], sum2[2][0], sum2[2][1], sum2[3][0], sum2[3][1]};
            const int row = (tid * 13108) >> 16, f = (tid - 5 * row) * 8;
            LAS char* dst = kvb + (row / 5) * VROW + (row % 5) * 96;
            *(LAS u32x2*)(dst + (f >= 20 ? 8 + 2 * f : 2 * f)) = u32x2{pack2(sum[0], sum[1]), pack2(sum[2], sum[3])};
            *(LAS u32x2*)(dst + (f + 4 >= 20 ? 16 + 2 * f : 8 + 2 * f)) = u32x2{pack2(sum[4], sum[5]), pack2(sum[6], sum[7])};
        }
        BAR();

        if (w < 8) {
            const int row = head * 19 + (r31 < 19 ? r31 : 18);
            const LAS char* ap = kvb + (row / 5) * VROW + (row % 5) * 96 + 48 * h;
            const bf16x8 a0 = *(const LAS bf16x8*)(ap), a1 = *(const LAS bf16x8*)(ap + 16);
            const bf16x4 a2 = __builtin_bit_cast(bf16x4, u32x2{*(const LAS unsigned*)(ap + 32), 0u});
            f32x16 o[2];
#pragma unroll
            for (int tb = 0; tb < 2; ++tb) {
                o[tb] = mfma32(a0, __builtin_bit_cast(bf16x8, u32x4{pq[tb][0], pq[tb][1], pq[tb][2], pq[tb][3]}), f32x16{});
                o[tb] = mfma32(a1, __builtin_bit_cast(bf16x8, u32x4{pq[tb][4], pq[tb][5], pq[tb][6], pq[tb][7]}), o[tb]);
                o[tb] = mfma32k8(a2, __builtin_bit_cast(bf16x4, u32x2{pq[tb][8], 0u}), o[tb]);
            }
#pragma unroll
            for (int tb = 0; tb < 2; ++tb) write_o(ob, lane, head, 64 * th + 32 * tb, o[tb], gt[tb] * __builtin_amdgcn_rcpf(swap_lo(o[tb][10]) + 1e-6f));
        }
    }
    BAR();

    { const unsigned od = (unsigned)WS_O + ((br * 8 + g) * 2048 + r0) * 144;
#pragma unroll
      for (int j = 0; j < 2; ++j) { const int c = tid + 640 * j;
          if (c < 1152) __builtin_amdgcn_raw_buffer_store_b128(*(const LAS u32x4*)(ob + c * 16), rws, od + c * 16, 0, 16); } }
    if (tid == 576) *(LAS u32x2*)(lds + L_RDY) = u32x2{0u, 0u};
    WAITV(0);
    BAR();
    if (tid == 0) set_flag(p.ws + WS_FO + (rb * 16 + br * 8 + g) * 128, tag);

    if (bid == 255 && w == 8) {
        for (unsigned spins = 0;;) {
            bool ok = true;
#pragma unroll
            for (int j = 0; j < 4; ++j) ok &= __hip_atomic_load((const GAS u64*)(p.ws + (j < 2 ? WS_FKV : WS_FKP - 16384) + (lane + 64 * j) * 128), __ATOMIC_RELAXED, __HIP_MEMORY_SCOPE_AGENT) == tag;
            if (__all(ok)) { if (lane == 0) set_flag(p.ws + WS_EPOCH, tag); break; }
            if (++spins > SPIN_CAP) break;
            __builtin_amdgcn_s_sleep(1);
        }
    }
    { const int xcd = bid & 7, ii = bid >> 3, t = xcd * 31 + ii;
      if (ii < 31 && t < 243) { phase3(p, lds, tid, lane, w, (t / 9) * P3_R, t % 9, tag); } }
    asm volatile("k_fused_code_end:");
}

extern "C" void kernel_launch(void* const* d_in, const int* in_sizes, int n_in, void* d_out, int out_size, void* d_ws, size_t ws_size, hipStream_t stream) {
    Params p;
    p.x = d_in[0]; p.wqkvA = d_in[1]; p.wphi = d_in[2]; p.woA = d_in[3]; p.wqkvB = d_in[5]; p.woB = d_in[6]; p.gw = d_in[7];
    p.out = d_out; p.ws = (char*)d_ws;
    hipFuncSetAttribute((const void*)k_fused, hipFuncAttributeMaxDynamicSharedMemorySize, LDS_BYTES);
    k_fused<<<256, 640, LDS_BYTES, stream>>>(p);
}
```
